```python
import torch

def _softmax(x, axis=-1):
    return torch.softmax(x, dim=axis)

def _gelu(x):
    return torch.nn.functional.gelu(x, approximate='tanh')

def _silu(x):
    return torch.nn.functional.silu(x)

def _sigmoid(x):
    return torch.sigmoid(x)

def _layernorm(x, axis=-1, eps=1e-05):
    m = x.mean(dim=axis, keepdim=True)
    v = x.var(dim=axis, keepdim=True, unbiased=False)
    return (x - m) / torch.sqrt(v + eps)

def _scatter_add(tgt, idx, src, axis=0):
    return tgt.clone().index_add_(axis, idx, src)

def _segment_sum(src, seg, n):
    out = torch.zeros((n,) + src.shape[1:], dtype=src.dtype)
    return out.index_add_(0, seg, src)

def _pad(x, pw):
    flat = tuple((v for pair in reversed(pw) for v in pair))
    return torch.nn.functional.pad(x, flat)
kernel_name = 'adaLN_cond__sliding_block__moe_arch_mod__prompt'

def setup_inputs():

    _w = lambda a: (lambda t: t.to(torch.bfloat16) if t.is_floating_point() else t)(torch.as_tensor(a))

    def _A_setup():
        B, S, D = (1, 2048, 512)
        return {'x': _w((lambda *s: torch.randn(*s, dtype=torch.bfloat16) * 0.05)(B, S, D)), 'wqkv': _w((lambda *s: torch.randn(*s, dtype=torch.bfloat16) * 0.05)(D, 3 * D)), 'wo': _w((lambda *s: torch.randn(*s, dtype=torch.bfloat16) * 0.05)(D, D))}
    _A_in = _A_setup()

    def _B_setup():
        B, S, D, F, C = (1, 2048, 512, 1792, 2)
        return {'x': _w((lambda *s: torch.randn(*s, dtype=torch.bfloat16) * 0.05)(B, S, D)), 'w_r': _w((lambda *s: torch.randn(*s, dtype=torch.bfloat16) * 0.05)(D)), 'w_gate': _w((lambda *s: torch.randn(*s, dtype=torch.bfloat16) * 0.05)(D, F)), 'w_up': _w((lambda *s: torch.randn(*s, dtype=torch.bfloat16) * 0.05)(D, F)), 'w_down': _w((lambda *s: torch.randn(*s, dtype=torch.bfloat16) * 0.05)(F, D))}
    _B_in = _B_setup()
    return {'A_x': _A_in['x'], 'A_wqkv': _A_in['wqkv'], 'A_wo': _A_in['wo'], 'B_x': _B_in['x'], 'B_w_r': _B_in['w_r'], 'B_w_gate': _B_in['w_gate'], 'B_w_up': _B_in['w_up'], 'B_w_down': _B_in['w_down'], 'c': _w((lambda *s: torch.randn(*s, dtype=torch.bfloat16) * 0.1)(1, 512) * 8.0), 'w_mod': _w((lambda *s: torch.randn(*s, dtype=torch.bfloat16) * 0.1)(512, 6 * 512) * 512 ** (-0.5) * 8.0)}

def reference(A_x, A_wqkv, A_wo, B_x, B_w_r, B_w_gate, B_w_up, B_w_down, c, w_mod):

    def _A_ref(x, wqkv, wo):
        B, S, D = x.shape
        H = 32
        Dh = D // H
        W = 256
        qkv = (lambda x, *d: x.permute(*d))((x @ wqkv).reshape(B, S, 3, H, Dh), 2, 0, 3, 1, 4)
        q, k, v = (qkv[0], qkv[1], qkv[2])
        scores = torch.einsum('bhqd,bhkd->bhqk', q, k) * Dh ** (-0.5)
        idx = torch.arange(S)
        mask = (idx[:, None] - idx[None, :] >= 0) & (idx[:, None] - idx[None, :] < W)
        scores = torch.where(mask[None, None], scores, -1000000000.0)
        p = _softmax(scores, axis=-1)
        o = torch.einsum('bhqk,bhkd->bhqd', p, v)
        return (lambda x, *d: x.permute(*d))(o, 0, 2, 1, 3).reshape(B, S, D) @ wo

    def _B_ref(x, w_r, w_gate, w_up, w_down):
        B, S, D = x.shape
        C = 2
        r = x @ w_r
        idx = torch.argsort(-r, axis=-1)[:, :C]
        idx_e = (lambda x, n, axis: torch.repeat_interleave(x, n, dim=axis))(idx.reshape(B, C, 1), D, axis=2)
        x_sel = (lambda x, i, axis: torch.take_along_dim(x, i, dim=axis))(x, idx_e, axis=1)
        g = _silu(x_sel @ w_gate)
        u = x_sel @ w_up
        y = g * u @ w_down
        r_sel = _sigmoid((lambda x, i, axis: torch.take_along_dim(x, i, dim=axis))(r, idx, axis=1))
        y = y * r_sel.reshape(B, C, 1)
        b_off = (torch.arange(B) * S).reshape(B, 1)
        flat_idx = (idx + b_off).reshape(B * C)
        zero = (x * 0.0).reshape(B * S, D)
        upd = _scatter_add(zero, flat_idx, y.reshape(B * C, D), axis=0)
        return x + upd.reshape(B, S, D)
    x = A_x
    mod = (_silu(c) @ w_mod).reshape(c.shape[0], 6, 1, -1)
    sa, ca, ga, sb, cb, gb = (mod[:, 0].clone(), mod[:, 1].clone(), mod[:, 2].clone(), mod[:, 3].clone(), mod[:, 4].clone(), mod[:, 5].clone())
    h = _layernorm(x, axis=-1) * (1 + ca) + sa
    x = x + ga * _A_ref(h, A_wqkv, A_wo)
    h = _layernorm(x, axis=-1) * (1 + cb) + sb
    x = x + gb * _B_ref(h, B_w_r, B_w_gate, B_w_up, B_w_down)
    return x
```

```cpp
#include <hip/hip_runtime.h>
#include <hip/hip_bf16.h>
typedef unsigned short u16;
__device__ __forceinline__ float bf2f(u16 v){ return __uint_as_float(((unsigned)v)<<16); }
__device__ __forceinline__ u16 f2bf(float f){ __hip_bfloat16 b = __float2bfloat16(f); return *(u16*)&b; }
__device__ __forceinline__ float rb(float f){ return bf2f(f2bf(f)); }
constexpr int S=2048, D=512, F=1792, H=32, DH=16, W=256;

__global__ void k_mod(const u16* c, const u16* wmod, float* mod){
  int n = blockIdx.x*64+threadIdx.x;
  float acc=0;
  for(int k=0;k<D;k++){ float cv=bf2f(c[k]); float s=rb(cv/(1.f+__expf(-cv))); acc+=s*bf2f(wmod[k*3072+n]); }
  mod[n]=rb(acc);
}
__device__ float block_sum(float v, float* red){
  for(int m=32;m>=1;m>>=1) v+=__shfl_xor(v,m);
  __syncthreads();
  if((threadIdx.x&63)==0) red[threadIdx.x>>6]=v;
  __syncthreads();
  float s=0; for(int i=0;i<(int)(blockDim.x>>6);i++) s+=red[i];
  return s;
}
__global__ void k_qkv(const u16* x, const float* mod, const u16* wqkv, u16* qkv){
  __shared__ float h[D]; __shared__ float red[8];
  int t=blockIdx.x, k=threadIdx.x;
  float xv=bf2f(x[t*D+k]);
  float m=block_sum(xv,red)/D; float d=xv-m; float v=block_sum(d*d,red)/D;
  float hn=d*rsqrtf(v+1e-5f);
  h[k]=rb(hn*rb(1.f+mod[512+k])+mod[k]);
  __syncthreads();
  for(int j=0;j<3;j++){ int n=j*512+k; float acc=0; for(int kk=0;kk<D;kk++) acc+=h[kk]*bf2f(wqkv[kk*1536+n]); qkv[t*1536+n]=f2bf(acc);}
}
__global__ void k_attn(const u16* x, const u16* qkv, const float* mod, const u16* wo, const u16* wr, float* x1o, float* h2o, float* ro, u16* out){
  __shared__ float p[W]; __shared__ float o[D]; __shared__ float red[8];
  int t=blockIdx.x, j=threadIdx.x;
  for(int h=0;h<H;h++){
    int key=t-j; float s=-1e30f;
    if(key>=0){ s=0; for(int d=0;d<DH;d++) s+=bf2f(qkv[t*1536+h*16+d])*bf2f(qkv[key*1536+512+h*16+d]); s*=0.25f; }
    float mx=s; for(int m=32;m>=1;m>>=1) mx=fmaxf(mx,__shfl_xor(mx,m));
    __syncthreads(); if((j&63)==0) red[j>>6]=mx; __syncthreads();
    mx=fmaxf(fmaxf(red[0],red[1]),fmaxf(red[2],red[3]));
    float e= key>=0? __expf(s-mx):0.f;
    float sum=block_sum(e,red);
    p[j]=e/sum; __syncthreads();
    if(j<16){ float acc=0; for(int i=0;i<W && i<=t;i++) acc+=p[i]*bf2f(qkv[(t-i)*1536+1024+h*16+j]); o[h*16+j]=rb(acc);}
    __syncthreads();
  }
  float x1[2];
  for(int u=0;u<2;u++){ int n=u*256+j; float acc=0; for(int k=0;k<D;k++) acc+=o[k]*bf2f(wo[k*D+n]); x1[u]=rb(bf2f(x[t*D+n])+rb(mod[1024+n]*rb(acc))); }
  float m=block_sum(x1[0]+x1[1],red)/D; float d0=x1[0]-m,d1=x1[1]-m; float v=block_sum(d0*d0+d1*d1,red)/D; float rs=rsqrtf(v+1e-5f);
  float rr=0;
  for(int u=0;u<2;u++){ int n=u*256+j; float h2=rb((x1[u]-m)*rs*rb(1.f+mod[2048+n])+mod[1536+n]); rr+=h2*bf2f(wr[n]); x1o[t*D+n]=x1[u]; h2o[t*D+n]=h2; out[t*D+n]=f2bf(x1[u]+rb(mod[2560+n]*h2)); }
  rr=block_sum(rr,red); if(j==0) ro[t]=rr;
}
__device__ __forceinline__ void top2(const float* r, int* idx){
  float b0=-INFINITY,b1=-INFINITY; int i0=0,i1=1;
  for(int i=0;i<S;i++){ float v=r[i]; bool g0=v>b0, g1=v>b1; i1=g0?i0:(g1?i:i1); b1=g0?b0:(g1?v:b1); i0=g0?i:i0; b0=g0?v:b0; }
  idx[0]=i0; idx[1]=i1;
}
__global__ void k_gu(const float* r, const float* h2, const u16* wg, const u16* wu, float* gu){
  __shared__ int idx[2];
  if(threadIdx.x==0){top2(r,idx);} __syncthreads();
  int f=blockIdx.x*64+threadIdx.x;
  for(int c=0;c<2;c++){ float g=0,u=0; for(int k=0;k<D;k++){ float hv=h2[idx[c]*D+k]; g+=hv*bf2f(wg[k*F+f]); u+=hv*bf2f(wu[k*F+f]); } g=rb(g); u=rb(u); g=rb(g/(1.f+__expf(-g))); gu[c*F+f]=rb(g*u);}
}
__global__ void k_down(const float* r, const float* h2, const float* x1, const float* mod, const float* gu, const u16* wd, u16* out){
  __shared__ int idx[2];
  if(threadIdx.x==0){top2(r,idx);} __syncthreads();
  int n=blockIdx.x*64+threadIdx.x;
  for(int c=0;c<2;c++){ float y=0; for(int f=0;f<F;f++) y+=gu[c*F+f]*bf2f(wd[f*D+n]); y=rb(y); float rv=rb(r[idx[c]]); y=rb(y*rb(1.f/(1.f+__expf(-rv)))); int t=idx[c]; out[t*D+n]=f2bf(x1[t*D+n]+rb(mod[2560+n]*rb(h2[t*D+n]+y))); }
}
extern "C" void kernel_launch(void* const* d_in, const int* in_sizes, int n_in, void* d_out, int out_size, void* d_ws, size_t ws_size, hipStream_t stream){
  const u16* x=(const u16*)d_in[0]; const u16* wqkv=(const u16*)d_in[1]; const u16* wo=(const u16*)d_in[2];
  const u16* wr=(const u16*)d_in[4]; const u16* wg=(const u16*)d_in[5]; const u16* wu=(const u16*)d_in[6]; const u16* wd=(const u16*)d_in[7];
  const u16* c=(const u16*)d_in[8]; const u16* wmod=(const u16*)d_in[9];
  char* ws=(char*)d_ws;
  float* mod=(float*)ws; u16* qkv=(u16*)(ws+(1<<20)); float* x1=(float*)(ws+(8<<20)); float* h2=(float*)(ws+(16<<20)); float* r=(float*)(ws+(24<<20)); float* gu=(float*)(ws+(25<<20));
  u16* out=(u16*)d_out;
  k_mod<<<48,64,0,stream>>>(c,wmod,mod);
  k_qkv<<<S,512,0,stream>>>(x,mod,wqkv,qkv);
  k_attn<<<S,256,0,stream>>>(x,qkv,mod,wo,wr,x1,h2,r,out);
  k_gu<<<F/64,64,0,stream>>>(r,h2,wg,wu,gu);
  k_down<<<8,64,0,stream>>>(r,h2,x1,mod,gu,wd,out);
}
```

```cpp
#include <hip/hip_runtime.h>
#include <hip/hip_bf16.h>

typedef unsigned short u16; typedef unsigned u32; typedef unsigned long long u64;
typedef u32 v2u __attribute__((ext_vector_type(2)));
typedef u32 v4u __attribute__((ext_vector_type(4)));
typedef short v4s __attribute__((ext_vector_type(4)));
typedef float f2 __attribute__((ext_vector_type(2)));
typedef float f4 __attribute__((ext_vector_type(4)));
typedef float f16v __attribute__((ext_vector_type(16)));
typedef __bf16 bf2 __attribute__((ext_vector_type(2)));
typedef __bf16 bf8 __attribute__((ext_vector_type(8)));
#define DI __device__ __forceinline__
#define LAS __attribute__((address_space(3)))

constexpr u32 WS_EPOCH = 0;
constexpr u32 WS_PAD   = 0x200;
constexpr u32 WS_MODP  = 0x1000;
constexpr u32 WS_CAND  = 0x20000;
constexpr u32 WS_BC    = 0x28000;
constexpr u32 WS_H2C   = 0x30000;
constexpr u32 WS_ST    = 0x50000;
constexpr u32 WS_KVCS  = 0x90000;
constexpr u32 WS_OCS   = 0xD0000;
constexpr u32 WS_BASE  = 0x110000;
constexpr u32 WS_YP    = 0x190000;
constexpr u32 WS_KV    = 0x200000;
constexpr u32 KV_TS    = 65536 + 256;
constexpr u32 WS_O     = 0x700000;
constexpr u32 O_TS     = 32768 + 256;
constexpr u32 WS_DIAG  = 0x4000000;

constexpr u32 L_R0 = 0;
constexpr u32 L_R1 = 65536;
constexpr u32 L_KO = L_R1 + 67584;
constexpr u32 L_VO = L_KO + 8192;
constexpr u32 L_QO = L_VO + 8192;
constexpr u32 L_SC = L_QO + 8192;
constexpr u32 L_SH = L_SC + 2048;
constexpr u32 L_MISC = L_SH + 2048;
constexpr u32 L_CNT = L_MISC;
constexpr u32 L_RR  = L_MISC + 64;
constexpr u32 L_ACT = L_MISC + 512;
static_assert(L_MISC + 2048 == 163840, "lds");
constexpr u32 H_FS = 1056, H_MS = 32 * 1056;
constexpr int SPIN_CAP = 4096;
constexpr u32 ONES = 0x3F803F80u;

__shared__ __attribute__((aligned(16))) char smem[163840];
template <class T> DI LAS T* lds(u32 off) { return (LAS T*)((LAS char*)smem + off); }

DI float lo2f(u32 v) { return __uint_as_float(v << 16); }
DI float hi2f(u32 v) { return __uint_as_float(v & 0xffff0000u); }
DI u32 pk(float a, float b) { f2 x = {a, b}; return __builtin_bit_cast(u32, __builtin_convertvector(x, bf2)); }
DI float rbf(float a) { return lo2f(pk(a, a)); }
DI float dot2(u32 a, u32 b, float c) { return __builtin_amdgcn_fdot2_f32_bf16(__builtin_bit_cast(bf2, a), __builtin_bit_cast(bf2, b), c, false); }
DI f16v mfma32(v4u a, v4u b, f16v c) { return __builtin_amdgcn_mfma_f32_32x32x16_bf16(__builtin_bit_cast(bf8, a), __builtin_bit_cast(bf8, b), c, 0, 0, 0); }
DI f4 mfma16(v4u a, v4u b, f4 c) { return __builtin_amdgcn_mfma_f32_16x16x32_bf16(__builtin_bit_cast(bf8, a), __builtin_bit_cast(bf8, b), c, 0, 0, 0); }
DI v2u trd(u32 off) { return __builtin_bit_cast(v2u, __builtin_amdgcn_ds_read_tr16_b64_v4i16(lds<v4s>(off))); }
DI v4u trd2(u32 o0, u32 o1) { v2u a = trd(o0), b = trd(o1); return (v4u){a.x, a.y, b.x, b.y}; }
template <int C> DI u32 dppu(u32 v) { return (u32)__builtin_amdgcn_update_dpp(0, (int)v, C, 0xf, 0xf, true); }
template <int C> DI float dppf(float v) { return __uint_as_float(dppu<C>(__float_as_uint(v))); }
constexpr int ROR8 = 0x128, ROR4 = 0x124, ROR2 = 0x122, ROR1 = 0x121, QX1 = 0xB1, QX2 = 0x4E, HMIR = 0x141;
DI u32 rowsum(u32 v) { v += dppu<ROR8>(v); v += dppu<ROR4>(v); v += dppu<ROR2>(v); v += dppu<ROR1>(v); return v; }
DI float rowsum(float v) { v += dppf<ROR8>(v); v += dppf<ROR4>(v); v += dppf<ROR2>(v); v += dppf<ROR1>(v); return v; }
DI float sum8(float v) { v += dppf<QX1>(v); v += dppf<QX2>(v); v += dppf<HMIR>(v); return v; }
DI u32 x16(u32 v) { auto r = __builtin_amdgcn_permlane16_swap(v, v, false, false); return r[0] + r[1]; }
DI u32 x32(u32 v) { auto r = __builtin_amdgcn_permlane32_swap(v, v, false, false); return r[0] + r[1]; }
DI float x16f(float v) { u32 u = __float_as_uint(v); auto r = __builtin_amdgcn_permlane16_swap(u, u, false, false); return __uint_as_float(r[0]) + __uint_as_float(r[1]); }
DI float x32f(float v) { u32 u = __float_as_uint(v); auto r = __builtin_amdgcn_permlane32_swap(u, u, false, false); return __uint_as_float(r[0]) + __uint_as_float(r[1]); }
DI float m16f(float v) { u32 u = __float_as_uint(v); auto r = __builtin_amdgcn_permlane16_swap(u, u, false, false); return fmaxf(__uint_as_float(r[0]), __uint_as_float(r[1])); }
DI float m32f(float v) { u32 u = __float_as_uint(v); auto r = __builtin_amdgcn_permlane32_swap(u, u, false, false); return fmaxf(__uint_as_float(r[0]), __uint_as_float(r[1])); }
DI float wavemax(float v) { v = fmaxf(v, dppf<ROR8>(v)); v = fmaxf(v, dppf<ROR4>(v)); v = fmaxf(v, dppf<ROR2>(v)); v = fmaxf(v, dppf<ROR1>(v)); return m32f(m16f(v)); }
DI u32 sum4(v4u v) { return v.x + v.y + v.z + v.w; }
DI float ex2(float v) { return __builtin_amdgcn_exp2f(v); }
DI float sigm(float v) { return __builtin_amdgcn_rcpf(1.f + ex2(-1.4426950408889634f * v)); }
DI void top2(float r, int lane, int& i1, int& i2) {
  float m1 = wavemax(r); u64 b1 = __ballot(r == m1); i1 = b1 ? __builtin_ctzll(b1) : 0;
  float r2 = lane == i1 ? -__builtin_inff() : r;
  float m2 = wavemax(r2); u64 b2 = __ballot(r2 == m2) & ~(1ull << i1); i2 = b2 ? __builtin_ctzll(b2) : (i1 ^ 1);
}

#define LDX(name, T, ins, fl) template <int O = 0> DI void name(T& d, u32 vo, u64 sb) { asm volatile(ins " %0, %1, %2 offset:%3" fl : "=v"(d) : "v"(vo), "s"(sb), "i"(O)); }
LDX(ld4, v4u, "global_load_dwordx4", "") LDX(ld4c, v4u, "global_load_dwordx4", " sc1") LDX(ld2c, v2u, "global_load_dwordx2", " sc1")
LDX(ld1, u32, "global_load_dword", "") LDX(ld1c, u32, "global_load_dword", " sc1") LDX(ldh, u32, "global_load_ushort", "")
#define STX(name, T, ins) template <int O = 0> DI void name(u32 vo, T d, u64 sb) { asm volatile(ins " %0, %1, %2 offset:%3 sc1\n\ts_nop 1" :: "v"(vo), "v"(d), "s"(sb), "i"(O) : "memory"); }
STX(st4c, v4u, "global_store_dwordx4") STX(st2c, v2u, "global_store_dwordx2") STX(st1c, u32, "global_store_dword")
#define STM(name, T, ins) template <int O = 0> DI void name(u64 mask, u32 vo, T d, u64 sb) { asm volatile("s_mov_b64 exec, %4\n\t" ins " %0, %1, %2 offset:%3 sc1\n\ts_mov_b64 exec, -1\n\ts_nop 0" :: "v"(vo), "v"(d), "s"(sb), "i"(O), "s"(mask) : "memory"); }
STM(sm4c, v4u, "global_store_dwordx4") STM(sm2c, v2u, "global_store_dwordx2") STM(sm1c, u32, "global_store_dword")
DI void dma(u32 vo, u64 sb, u32 l) { asm volatile("s_mov_b32 m0, %2\n\ts_nop 0\n\tglobal_load_lds_dwordx4 %0, %1" :: "v"(vo), "s"(sb), "s"(l) : "memory"); }
template <int N> DI void vmw() { asm volatile("s_waitcnt vmcnt(%0)" :: "i"(N) : "memory"); }
#define PIN(x) asm volatile("" : "+v"(x))
#define BAR() asm volatile("s_waitcnt lgkmcnt(0)\n\ts_barrier" ::: "memory")
#define SB0() __builtin_amdgcn_sched_barrier(0)
#define STAMP(k) do {} while (0)

__global__ void __launch_bounds__(512, 2)
fused_kernel(const char* __restrict__ gx, const char* __restrict__ gwqkv, const char* __restrict__ gwo, const char* __restrict__ gwr,
             const char* __restrict__ gwg, const char* __restrict__ gwu, const char* __restrict__ gwd, const char* __restrict__ gc,
             const char* __restrict__ gwmod, char* gout, char* gws) {
#define NP(k) do {} while (0)
  STAMP(0);
  const int tid = threadIdx.x, lane = tid & 63, w = __builtin_amdgcn_readfirstlane(tid >> 6);
  const int b = blockIdx.x, xcd = b & 7, bi = b >> 3;
  u32 pf0;
  {
    u64 pc; u32 csz;
    asm volatile("s_getpc_b64 %0\nKSTART%=:\n\ts_mov_b32 %1, KERNEL_END_MARK-KSTART%=" : "=s"(pc), "=s"(csz));
    u32 o = (u32)(bi * 8 + (lane & 7)) * 128u; o = o < csz ? o : 0u;
    asm volatile("s_cmp_lg_u32 %3, 3\n\ts_cbranch_scc1 1f\n\tglobal_load_dword %0, %1, %2\n1:" : "=v"(pf0) : "v"(o), "s"(pc), "s"(w) : "scc");
  }
  const u64 X = (u64)gx, WQKV = (u64)gwqkv, WO = (u64)gwo, WS = (u64)gws, OUT = (u64)gout;
  const int ct = (xcd >> 1) * 8 + (bi >> 2), hq = (xcd & 1) * 4 + (bi & 3);
  const int rot = (bi + (bi >> 2)) & 3, rh = (bi >> 4) & 1;
  const int wt = (xcd >> 1) * 8 + (xcd & 1) * 4 + (bi >> 3), sl = bi & 7;
  u32 vE; ld1<WS_EPOCH>(vE, 0u, WS);
  u32 tag;
  const u32 wvo = (u32)(lane >> 3) * 3072u + (u32)(((lane & 7) ^ ((lane >> 2) & 4)) * 16);
  const int h = lane >> 5;
  const u32 trq = (u32)((lane & 15) >> 2), trb = trq * 128u + ((((u32)(lane >> 4) & 1u) * 2u + ((u32)(lane & 3) >> 1)) * 16u) + (u32)(lane & 1) * 8u;
  v4u hf[32];

  if (w >= 4) {
    const int mw = w - 4, m = mw >> 1, p = mw & 1;
    __builtin_amdgcn_s_setprio(1);
    const bool prod = b < 192;
    const int cg = b < 64 ? (b & 15) : 16 + ((b - 64) & 31), rb = b < 64 ? (b >> 4) : ((b - 64) >> 5);
    v4u wm[4]; u32 cv, pf1;
    {
      const u64 cbase = (u64)gc + (u32)(rb * 128 + mw * 32) * 2u, mbase = (u64)gwmod + (u32)(rb * 128 + mw * 32) * 6144u + (u32)cg * 128u;
      const u32 co = (u32)((lane >> 3) * 4 + (lane & 3)) * 2u, mo = (u32)(lane >> 3) * 4u * 6144u + (u32)(lane & 7) * 16u;
      asm volatile("s_cmp_lt_u32 %11, 192\n\ts_cbranch_scc0 1f\n\tglobal_load_ushort %4, %5, %6\n\tglobal_load_dwordx4 %0, %7, %10 offset:-3072\n\tglobal_load_dwordx4 %1, %7, %10 offset:3072\n\t"
                   "global_load_dwordx4 %2, %8, %10 offset:-3072\n\tglobal_load_dwordx4 %3, %8, %10 offset:3072\n1:"
                   : "=&v"(wm[0]), "=&v"(wm[1]), "=&v"(wm[2]), "=&v"(wm[3]), "=&v"(cv)
                   : "v"(co), "s"(cbase), "v"(mo), "v"(mo + 12288u), "v"(0), "s"(mbase + 3072u), "s"(b) : "scc");
    }
    const u64 wcol = WQKV + (u32)hq * 128u;
    auto wpiece = [&](int n, u32 part, u32 lbase) {
      const int j = n >> 2, pc = (((n >> 1) & 1) ^ rh) * 8 + (n & 1) * 4 + (w & 3);
      dma(wvo, wcol + part + (u32)((((j + rot) & 3) * 16 + pc) * 8) * 3072u, lbase + (u32)(j * 16 + pc) * 1024u);
    };
    wpiece(0, 1024u, L_R0); wpiece(1, 1024u, L_R0);
    {
      u64 a = wcol + (mw == 0 ? 2048u : 0u) + (u32)((ct & 7) * 64 + lane) * 3072u;
      if (mw >= 2) {
        const int t = lane & 15;
        a = WS + WS_KV + 65536u + (u32)((ct * 2 + 56 + t) & 63) * KV_TS;
        if (t == 10) a = WS + WS_O + 32768u + (u32)(ct * 2) * O_TS;
        if (t == 11) a = WS + WS_O + 32768u + (u32)(wt * 2) * O_TS;
        if (t == 12) a = WS + WS_PAD;
        if (t == 13) a = WO;
        if (t == 14) a = (u64)gwr;
        if (t == 15) a = OUT + (u32)(wt * 64) * 1024u + (u32)sl * 128u;
      }
      asm volatile("global_load_dword %0, %1, off" : "=v"(pf1) : "v"(a));
    }
    __builtin_amdgcn_s_setprio(0);
    if (tid == 256) *lds<u32>(L_CNT) = 0u;
    BAR();
    if (prod) {
      asm volatile("s_waitcnt vmcnt(3)" : "+v"(wm[0]), "+v"(wm[1]), "+v"(wm[2]), "+v"(wm[3]), "+v"(cv), "+v"(vE));
      STAMP(1);
      const float cf = lo2f(cv); const u32 sl_ = pk(cf * sigm(cf), 0.f);
      float a[8] = {0, 0, 0, 0, 0, 0, 0, 0};
#define MODROW(u) { const u32 s0 = dppu<u * 0x55>(sl_), s1 = s0 << 16; \
        a[0] = dot2(wm[u].x, s0, a[0]); a[1] = dot2(wm[u].x, s1, a[1]); a[2] = dot2(wm[u].y, s0, a[2]); a[3] = dot2(wm[u].y, s1, a[3]); \
        a[4] = dot2(wm[u].z, s0, a[4]); a[5] = dot2(wm[u].z, s1, a[5]); a[6] = dot2(wm[u].w, s0, a[6]); a[7] = dot2(wm[u].w, s1, a[7]); }
      MODROW(0) MODROW(1) MODROW(2) MODROW(3)
      float q4[4], q2[2];
#pragma unroll
      for (int e = 0; e < 4; ++e) { auto r = __builtin_amdgcn_permlane32_swap(__float_as_uint(a[e]), __float_as_uint(a[e + 4]), false, false); q4[e] = __uint_as_float(r[0]) + __uint_as_float(r[1]); }
#pragma unroll
      for (int e = 0; e < 2; ++e) { auto r = __builtin_amdgcn_permlane16_swap(__float_as_uint(q4[e]), __float_as_uint(q4[e + 2]), false, false); q2[e] = __uint_as_float(r[0]) + __uint_as_float(r[1]); }
      q2[0] += dppf<ROR8>(q2[0]); q2[1] += dppf<ROR8>(q2[1]);
      const float val = (lane & 8) ? q2[1] : q2[0];
      const int col = 8 * (lane & 7) + 4 * (lane >> 5) + 2 * ((lane >> 4) & 1) + ((lane >> 3) & 1);
      *lds<float>(L_R1 + (u32)(mw * 64 + col) * 4u) = val;
      u32 old = 0;
      asm volatile("s_waitcnt lgkmcnt(0)" ::: "memory");
      if (lane == 0) old = __hip_atomic_fetch_add(lds<u32>(L_CNT), 1u, __ATOMIC_RELAXED, __HIP_MEMORY_SCOPE_WORKGROUP);
      if (__builtin_amdgcn_readfirstlane(old) == 3u) {
        asm volatile("" ::: "memory");
        const float s = (*lds<float>(L_R1 + lane * 4) + *lds<float>(L_R1 + 256 + lane * 4)) + (*lds<float>(L_R1 + 512 + lane * 4) + *lds<float>(L_R1 + 768 + lane * 4));
        st2c((u32)lane * 8u, (v2u){__float_as_uint(s), vE + 1u}, WS + WS_MODP + (u32)(rb * 3072 + cg * 64) * 8u);
      }
      STAMP(2);
    }
    for (int n = 2; n < 16; ++n) wpiece(n, 1024u, L_R0);
    asm volatile("s_waitcnt vmcnt(0)" : "+v"(pf1), "+v"(vE) :: "memory");
    tag = __builtin_amdgcn_readfirstlane(vE) + 1u;
    STAMP(3);
    BAR();
    const u32 tra = (trb ^ ((trq >> 1) * 64u) ^ ((u32)p * 64u)) + (u32)h * 1024u;
    v4u wf[32];
#pragma unroll
    for (int s = 0; s < 32; ++s) wf[s] = trd2(L_R0 + tra + s * 2048, L_R0 + tra + s * 2048 + 512);
    BAR();
    for (int n = 0; n < 16; ++n) wpiece(n, 2048u, L_R0);
    STAMP(4);
    BAR();
    const u32 ha = L_R1 + (u32)m * H_MS + (u32)h * 528u + (u32)(lane & 31) * 16u;
    f16v acc = {};
    BAR();
    STAMP(5);
#pragma unroll
    for (int s = 0; s < 8; ++s) hf[s] = *lds<v4u>(ha + s * H_FS);
#pragma unroll
    for (int j = 0; j < 4; ++j) {
#pragma unroll
      for (int s = 0; s < 6; ++s) acc = mfma32(wf[j * 8 + s], hf[j * 8 + s], acc);
      if (j < 3) {
        SB0(); BAR();
#pragma unroll
        for (int s = 8; s < 16; ++s) hf[j * 8 + s] = *lds<v4u>(ha + (j * 8 + s) * H_FS);
        SB0();
      }
#pragma unroll
      for (int s = 6; s < 8; ++s) acc = mfma32(wf[j * 8 + s], hf[j * 8 + s], acc);
    }
    const u64 kvb = WS + WS_KV + (u32)(ct * 2 + m) * KV_TS + (u32)(hq * 4 + p * 2) * 2048u;
    const u64 csb = WS + WS_KVCS + (u32)((ct * 2 + m) * 16 + hq * 2 + p) * 256u;
    {
      const v4u k0 = {pk(acc[0], acc[1]), pk(acc[2], acc[3]), pk(acc[4], acc[5]), pk(acc[6], acc[7])};
      const v4u k1 = {pk(acc[8], acc[9]), pk(acc[10], acc[11]), pk(acc[12], acc[13]), pk(acc[14], acc[15])};
      st4c<0>((u32)lane * 16u, k0, kvb); st4c<2048>((u32)lane * 16u, k1, kvb);
      auto r = __builtin_amdgcn_permlane32_swap(sum4(k0), sum4(k1), false, false);
      const u32 cs = x16(rowsum(r[0] + r[1]));
      sm2c(0x0000000F0000000Full, (u32)(h * 32 + (lane & 3) * 8), (v2u){cs, tag}, csb);
      *lds<v4u>(L_KO + (u32)(m * 4 + p * 2) * 1024u + lane * 16) = k0;
      *lds<v4u>(L_KO + (u32)(m * 4 + p * 2 + 1) * 1024u + lane * 16) = k1;
    }
    STAMP(6);
    vmw<3>();
    BAR();
    STAMP(7);
    acc = (f16v){};
#pragma unroll
    for (int s = 0; s < 32; ++s) acc = mfma32(hf[s], trd2(L_R0 + tra + s * 2048, L_R0 + tra + s * 2048 + 512), acc);
    {
      const v4u v0 = {pk(acc[0], acc[1]), pk(acc[2], acc[3]), pk(acc[4], acc[5]), pk(acc[6], acc[7])};
      const v4u v1 = {pk(acc[8], acc[9]), pk(acc[10], acc[11]), pk(acc[12], acc[13]), pk(acc[14], acc[15])};
      const u32 hb = (u32)(lane >> 4) & 1u, vo = (u32)h * 256u + (u32)(lane & 15) * 16u;
      st4c<1024>(hb * 2048u + vo, v0, kvb); st4c<1536>(hb * 2048u + vo, v1, kvb);
      const u32 cs = x32(rowsum(sum4(v0) + sum4(v1)));
      sm2c<128>(0x000F000Full, hb * 32u + (u32)(lane & 3) * 8u, (v2u){cs, tag}, csb);
      *lds<v4u>(L_VO + (u32)(m * 4 + p * 2) * 1024u + hb * 1024u + vo) = v0;
      *lds<v4u>(L_VO + (u32)(m * 4 + p * 2) * 1024u + hb * 1024u + vo + 512) = v1;
    }
    STAMP(8);
  } else {
    v4u xr[4][4];
    const u64 xb = X + (u32)(ct * 64 + w * 16) * 1024u + 4096u;
    const u32 xo = (u32)(lane >> 4) * 1024u + (u32)(lane & 15) * 16u;
#define LDXC(j) { const u64 sb = xb + (u32)(((j) + rot) & 3) * 256u; ld4<-4096>(xr[j][0], xo, sb); ld4<0>(xr[j][1], xo, sb); ld4<-4096>(xr[j][2], xo + 8192u, sb); ld4<0>(xr[j][3], xo + 8192u, sb); }
    LDXC(0)
    BAR();
    LDXC(1) LDXC(2) LDXC(3)
    STAMP(1);
    f2 xf[4][4][4]; float s1[4] = {0, 0, 0, 0}, s2[4] = {0, 0, 0, 0};
#define XPROC(j) { asm volatile("s_waitcnt vmcnt(%4)" : "+v"(xr[j][0]), "+v"(xr[j][1]), "+v"(xr[j][2]), "+v"(xr[j][3]) : "i"(12 - 4 * (j))); \
      _Pragma("unroll") for (int g = 0; g < 4; ++g) _Pragma("unroll") for (int e = 0; e < 4; ++e) { const u32 d = xr[j][g][e]; \
        s1[g] = dot2(d, ONES, s1[g]); s2[g] = dot2(d, d, s2[g]); xf[j][g][e] = (f2){lo2f(d), hi2f(d)}; } SB0(); }
    XPROC(0) XPROC(1) XPROC(2) XPROC(3)
    PIN(vE); PIN(pf0);
    tag = __builtin_amdgcn_readfirstlane(vE) + 1u;
    STAMP(2);
#pragma unroll
    for (int g = 0; g < 4; ++g) {
      const float mu = rowsum(s1[g]) * (1.f / 512.f), var = fmaxf(rowsum(s2[g]) * (1.f / 512.f) - mu * mu, 0.f);
      const float rs = __builtin_amdgcn_rsqf(var + 1e-5f), nb = -mu * rs;
#pragma unroll
      for (int j = 0; j < 4; ++j)
#pragma unroll
        for (int e = 0; e < 4; ++e) xf[j][g][e] = xf[j][g][e] * (f2){rs, rs} + (f2){nb, nb};
    }
    STAMP(3);
    BAR();
    {
      v4u g[8]; const u32 mo = (u32)tid * 16u; const u64 mb = WS + WS_MODP;
      bool first = true;
      for (int it = 0; it < SPIN_CAP; ++it) {
#pragma unroll
        for (int q = 0; q < 4; ++q) { ld4c<0>(g[q], mo, mb + q * 24576u); ld4c<0>(g[4 + q], mo + 4096u, mb + q * 24576u); }
        if (first) { BAR(); first = false; }
        asm volatile("s_waitcnt vmcnt(0)" : "+v"(g[0]), "+v"(g[1]), "+v"(g[2]), "+v"(g[3]), "+v"(g[4]), "+v"(g[5]), "+v"(g[6]), "+v"(g[7]));
        NP(0);
        u32 bad = 0;
#pragma unroll
        for (int q = 0; q < 8; ++q) bad |= (g[q].y ^ tag) | (g[q].w ^ tag);
        if (__all(bad == 0)) break;
      }
      const float a0 = (__uint_as_float(g[0].x) + __uint_as_float(g[1].x)) + (__uint_as_float(g[2].x) + __uint_as_float(g[3].x));
      const float a1 = (__uint_as_float(g[0].z) + __uint_as_float(g[1].z)) + (__uint_as_float(g[2].z) + __uint_as_float(g[3].z));
      const float c0 = (__uint_as_float(g[4].x) + __uint_as_float(g[5].x)) + (__uint_as_float(g[6].x) + __uint_as_float(g[7].x));
      const float c1 = (__uint_as_float(g[4].z) + __uint_as_float(g[5].z)) + (__uint_as_float(g[6].z) + __uint_as_float(g[7].z));
      const u32 sa = pk(a0, a1), ca = pk(c0, c1), sc = pk(1.f + lo2f(ca), 1.f + hi2f(ca));
      *lds<f2>(L_SH + tid * 8) = (f2){lo2f(sa), hi2f(sa)};
      *lds<f2>(L_SC + tid * 8) = (f2){lo2f(sc), hi2f(sc)};
    }
    STAMP(4);
    BAR();
    const u32 hw_ = L_R1 + (u32)(w >> 1) * H_MS + (u32)(lane & 15) * 528u + (u32)((w & 1) * 16 + (lane >> 4)) * 16u;
#pragma unroll
    for (int j = 0; j < 4; ++j) {
      const u32 ko = (u32)((j + rot) & 3) * 512u + (u32)(lane & 15) * 32u;
      const f4 c0 = *lds<f4>(L_SC + ko), c1 = *lds<f4>(L_SC + ko + 16), d0 = *lds<f4>(L_SH + ko), d1 = *lds<f4>(L_SH + ko + 16);
#pragma unroll
      for (int g = 0; g < 4; ++g) {
        const f2 y0 = xf[j][g][0] * c0.xy + d0.xy, y1 = xf[j][g][1] * c0.zw + d0.zw, y2 = xf[j][g][2] * c1.xy + d1.xy, y3 = xf[j][g][3] * c1.zw + d1.zw;
        *lds<v4u>(hw_ + j * 8 * H_FS + g * 64) = (v4u){pk(y0.x, y0.y), pk(y1.x, y1.y), pk(y2.x, y2.y), pk(y3.x, y3.y)};
      }
      SB0(); BAR();
    }
    STAMP(6);
    BAR();
    const u64 wcol = WQKV + (u32)hq * 128u;
    for (int n = 0; n < 16; ++n) {
      const int j = n >> 2, pc = (n & 3) * 4 + w;
      dma(wvo, wcol + (u32)((((j + rot) & 3) * 16 + pc) * 8) * 3072u, L_R1 + (u32)(j * 16 + pc) * 1024u);
    }
    vmw<0>();
    STAMP(8);
  }
  BAR();
  const int hh = w & 3, Y = w >> 2, head = hq * 4 + hh, Q0 = ct * 2;
  int tl[4]; bool ok[4];
#pragma unroll
  for (int j = 0; j < 4; ++j) { const int t = Q0 - 8 + (j == 0 ? Y : 1 + 3 * Y + j); ok[j] = t >= 0; tl[j] = ok[j] ? t : Q0 + Y; }
  const u64 kv0 = WS + WS_KV + (u32)head * 2048u;
  const u64 kb0 = kv0 + (u32)tl[0] * KV_TS, kb1 = kv0 + (u32)tl[1] * KV_TS, kb2 = kv0 + (u32)tl[2] * KV_TS, kb3 = kv0 + (u32)tl[3] * KV_TS;
  const int tsel = (lane & 2) ? ((lane & 1) ? tl[3] : tl[2]) : ((lane & 1) ? tl[1] : tl[0]);
  const u32 cso = (u32)(tsel * 16 + (head >> 1)) * 256u + (u32)(head & 1) * 32u;
  v4u kg[4], vg[4][2]; v2u kcs, vcs;
  const u32 l16 = (u32)lane * 16u, vlo = (u32)h * 256u + (u32)(lane & 15) * 16u;
#define LOADK() { ld4c<0>(kg[0], l16, kb0); ld4c<0>(kg[1], l16, kb1); ld4c<0>(kg[2], l16, kb2); ld4c<0>(kg[3], l16, kb3); ld2c<0>(kcs, cso, WS + WS_KVCS); }
#define LOADV() { asm volatile("s_mov_b64 exec, %13\n\tglobal_load_dwordx4 %0, %8, %9 offset:1024 sc1\n\tglobal_load_dwordx4 %1, %8, %9 offset:1536 sc1\n\t" \
      "global_load_dwordx4 %2, %8, %10 offset:1024 sc1\n\tglobal_load_dwordx4 %3, %8, %10 offset:1536 sc1\n\tglobal_load_dwordx4 %4, %8, %11 offset:1024 sc1\n\t" \
      "global_load_dwordx4 %5, %8, %11 offset:1536 sc1\n\tglobal_load_dwordx4 %6, %8, %12 offset:1024 sc1\n\tglobal_load_dwordx4 %7, %8, %12 offset:1536 sc1\n\ts_mov_b64 exec, -1" \
      : "+v"(vg[0][0]), "+v"(vg[0][1]), "+v"(vg[1][0]), "+v"(vg[1][1]), "+v"(vg[2][0]), "+v"(vg[2][1]), "+v"(vg[3][0]), "+v"(vg[3][1]) \
      : "v"(vlo), "s"(kb0), "s"(kb1), "s"(kb2), "s"(kb3), "s"(0x0000FFFF0000FFFFull)); ld2c<128>(vcs, cso, WS + WS_KVCS); }
  LOADK()
  if (w >= 4) {
    const int mw = w - 4, m = mw >> 1, p = mw & 1;
    const u32 tra = (trb ^ ((trq >> 1) * 64u) ^ ((u32)p * 64u)) + (u32)h * 1024u;
    f16v acc = {};
#pragma unroll
    for (int s = 0; s < 32; ++s) acc = mfma32(trd2(L_R1 + tra + s * 2048, L_R1 + tra + s * 2048 + 512), hf[s], acc);
    acc *= 0.36067376022224085f;
    *lds<v4u>(L_QO + (u32)(m * 4 + p * 2) * 1024u + lane * 16) = (v4u){pk(acc[0], acc[1]), pk(acc[2], acc[3]), pk(acc[4], acc[5]), pk(acc[6], acc[7])};
    *lds<v4u>(L_QO + (u32)(m * 4 + p * 2 + 1) * 1024u + lane * 16) = (v4u){pk(acc[8], acc[9]), pk(acc[10], acc[11]), pk(acc[12], acc[13]), pk(acc[14], acc[15])};
  }
#pragma unroll
  for (int j = 0; j < 4; ++j) { vg[j][0] = (v4u){ONES, ONES, ONES, ONES}; vg[j][1] = vg[j][0]; }
  STAMP(9);
  BAR();
  LOADV()
  const v4u q1 = *lds<v4u>(L_QO + (u32)(Y * 4 + hh) * 1024u + l16), q2 = *lds<v4u>(L_QO + (u32)((Y ^ 1) * 4 + hh) * 1024u + l16);
  const v4u kd = *lds<v4u>(L_KO + (u32)(Y * 4 + hh) * 1024u + l16);
  v4u vd0 = *lds<v4u>(L_VO + (u32)(Y * 4 + hh) * 1024u + vlo), vd1 = *lds<v4u>(L_VO + (u32)(Y * 4 + hh) * 1024u + vlo + 512);
  if (lane & 16) { vd0 = (v4u){ONES, ONES, ONES, ONES}; vd1 = vd0; }
  asm volatile("s_waitcnt vmcnt(9)" : "+v"(kg[0]), "+v"(kg[1]), "+v"(kg[2]), "+v"(kg[3]), "+v"(kcs));
  for (int it = 0; it < SPIN_CAP; ++it) {
    NP(1);
    const u32 t = x32(x16(rowsum(sum4(kg[0]) + sum4(kg[1]) + sum4(kg[2]) + sum4(kg[3]) - (lane < 4 ? kcs.x : 0u))));
    if (__all(t == 0u && kcs.y == tag)) break;
    LOADK()
    asm volatile("s_waitcnt vmcnt(0)" : "+v"(kg[0]), "+v"(kg[1]), "+v"(kg[2]), "+v"(kg[3]), "+v"(kcs));
  }
  STAMP(10);
  const int qi = lane & 31;
  const v4u kz = Y ? kg[0] : kd; const bool okz = Y ? ok[0] : true;
  constexpr float NEG = -1e30f;
  float m1, m2 = NEG; f16v sm;
#define MAX16(mm, s) { _Pragma("unroll") for (int e = 0; e < 16; e += 2) mm = fmaxf(fmaxf(mm, s[e]), s[e + 1]); }
  {
    const f16v z = {};
    const f16v sd = mfma32(kd, q1, z); f16v sg = mfma32(kg[0], q1, z);
    if (!ok[0]) sg = z + NEG;
#pragma unroll
    for (int e = 0; e < 16; ++e) sm[e] = ((e & 3) + 8 * (e >> 2) + 4 * h <= qi) ? sd[e] : sg[e];
    m1 = NEG; MAX16(m1, sm)
    if (okz) { const f16v s = mfma32(kz, q2, z); MAX16(m2, s) }
#pragma unroll
    for (int j = 1; j < 4; ++j) if (ok[j]) { const f16v a = mfma32(kg[j], q1, z), c = mfma32(kg[j], q2, z); MAX16(m1, a) MAX16(m2, c) }
    m1 = m32f(m1); m2 = m32f(m2);
  }
  STAMP(11);
  asm volatile("s_waitcnt vmcnt(0)" : "+v"(vg[0][0]), "+v"(vg[0][1]), "+v"(vg[1][0]), "+v"(vg[1][1]), "+v"(vg[2][0]), "+v"(vg[2][1]), "+v"(vg[3][0]), "+v"(vg[3][1]), "+v"(vcs));
  for (int it = 0; it < SPIN_CAP; ++it) {
    NP(2);
    const u32 t = x32(x16(rowsum(sum4(vg[0][0]) + sum4(vg[0][1]) + sum4(vg[1][0]) + sum4(vg[1][1]) + sum4(vg[2][0]) + sum4(vg[2][1]) + sum4(vg[3][0]) + sum4(vg[3][1]) - (lane < 4 ? vcs.x : 0u))));
    if (__all(t == 1024u * ONES && vcs.y == tag)) break;
    LOADV()
    asm volatile("s_waitcnt vmcnt(0)" : "+v"(vg[0][0]), "+v"(vg[0][1]), "+v"(vg[1][0]), "+v"(vg[1][1]), "+v"(vg[2][0]), "+v"(vg[2][1]), "+v"(vg[3][0]), "+v"(vg[3][1]), "+v"(vcs));
  }
  STAMP(12);
  v4u xres, wrv, m2g;
  const u32 m2o = (u32)(lane >> 4) * 24576u + (u32)(1024 + (w >> 1) * 512 + sl * 64 + (w & 1) * 32 + (lane & 15) * 2) * 8u;
  {
    const u32 ovo = (u32)(lane >> 3) * 1024u + (u32)(((lane & 7) ^ ((lane >> 2) & 4)) * 16);
    for (int n = 0; n < 8; ++n) { const int pc = ((n + (bi >> 3) * 2) & 7) * 8 + w; dma(ovo, WO + (u32)sl * 128u + (u32)pc * 8192u, L_R0 + (u32)pc * 1024u); }
    ld4<0>(xres, (u32)(tid >> 3) * 1024u + (u32)(tid & 7) * 16u, X + (u32)wt * 65536u + (u32)sl * 128u);
    ld4<0>(wrv, (u32)(tid & 7) * 16u, (u64)gwr + (u32)sl * 128u);
    ld4c<0>(m2g, m2o, WS + WS_MODP);
  }
  f16v o1 = {}, o2 = {};
  {
    f16v n1, n2;
#pragma unroll
    for (int e = 0; e < 16; ++e) { n1[e] = -m1; n2[e] = -m2; }
#define PKE(s, e) pk(ex2(s[e]), ex2(s[e + 1]))
#define PV(o, s, va, vb) { const v4u pa = {PKE(s, 0), PKE(s, 2), PKE(s, 4), PKE(s, 6)}, pb = {PKE(s, 8), PKE(s, 10), PKE(s, 12), PKE(s, 14)}; o = mfma32(va, pa, o); o = mfma32(vb, pb, o); }
    if (okz) { const f16v s = mfma32(kz, q2, n2); const v4u za = Y ? vg[0][0] : vd0, zb = Y ? vg[0][1] : vd1; PV(o2, s, za, zb) }
#pragma unroll
    for (int j = 1; j < 4; ++j) if (ok[j]) { const f16v a = mfma32(kg[j], q1, n1), c = mfma32(kg[j], q2, n2); PV(o2, c, vg[j][0], vg[j][1]) PV(o1, a, vg[j][0], vg[j][1]) }
    const u32 xo = L_R1 + (u32)w * 2560u;
    *lds<f4>(xo + l16) = (f4){o2[0], o2[1], o2[2], o2[3]}; *lds<f4>(xo + 1024 + l16) = (f4){o2[4], o2[5], o2[6], o2[7]};
    *lds<f2>(xo + 2048 + lane * 8) = (f2){o2[8], m2};
    sm -= m1;
    u32 pp[8], pd[8];
#pragma unroll
    for (int e = 0; e < 8; ++e) {
      pp[e] = PKE(sm, 2 * e);
      const int k0 = ((2 * e) & 3) + 8 * (e >> 1) + 4 * h;
      const u32 mk = (k0 <= qi ? 0xffffu : 0u) | (k0 + 1 <= qi ? 0xffff0000u : 0u);
      pd[e] = pp[e] & mk; pp[e] &= ~mk;
    }
    o1 = mfma32(vd0, (v4u){pd[0], pd[1], pd[2], pd[3]}, o1); o1 = mfma32(vd1, (v4u){pd[4], pd[5], pd[6], pd[7]}, o1);
    o1 = mfma32(vg[0][0], (v4u){pp[0], pp[1], pp[2], pp[3]}, o1); o1 = mfma32(vg[0][1], (v4u){pp[4], pp[5], pp[6], pp[7]}, o1);
  }
  asm volatile("s_waitcnt vmcnt(0)" : "+v"(xres), "+v"(wrv), "+v"(m2g) :: "memory");
  STAMP(13);
  BAR();
  {
    const u32 xo = L_R1 + (u32)(w ^ 4) * 2560u;
    const f4 pa = *lds<f4>(xo + l16), pb = *lds<f4>(xo + 1024 + l16); const f2 pl = *lds<f2>(xo + 2048 + lane * 8);
    const float mm = fmaxf(m1, pl.y); float fa = ex2(m1 - mm), fb = ex2(pl.y - mm);
    const float inv = __builtin_amdgcn_rcpf(fa * o1[8] + fb * pl.x); fa *= inv; fb *= inv;
    const v4u of = {pk(fa * o1[0] + fb * pa.x, fa * o1[1] + fb * pa.y), pk(fa * o1[2] + fb * pa.z, fa * o1[3] + fb * pa.w),
                    pk(fa * o1[4] + fb * pb.x, fa * o1[5] + fb * pb.y), pk(fa * o1[6] + fb * pb.z, fa * o1[7] + fb * pb.w)};
    st4c<0>(l16, of, WS + WS_O + (u32)(Q0 + Y) * O_TS + (u32)head * 1024u);
    const u32 cs = x32(x16(rowsum(sum4(of))));
    sm2c<0>(0xFFull, (u32)lane * 8u, (v2u){cs, tag}, WS + WS_OCS + (u32)((Q0 + Y) * 32 + head) * 128u);
  }
  STAMP(14);
  const int om = w & 1, kq = w >> 1;
  v4u bw[8][2];
#pragma unroll
  for (int s = 0; s < 8; ++s)
#pragma unroll
    for (int n = 0; n < 2; ++n) { const u32 a = L_R0 + (trb ^ ((trq >> 1) * 64u) ^ (n * 64u)) + (u32)h * 512u + (u32)kq * 16384u + s * 2048; bw[s][n] = trd2(a, a + 1024); }
  for (int it = 0; it < SPIN_CAP; ++it) {
    if (__all(m2g.y == tag && m2g.w == tag)) break;
    ld4c<0>(m2g, m2o, WS + WS_MODP); asm volatile("s_waitcnt vmcnt(0)" : "+v"(m2g));
  }
  {
    const u32 v = pk(x32f(x16f(__uint_as_float(m2g.x))), x32f(x16f(__uint_as_float(m2g.z))));
    const u32 v1 = pk(1.f + lo2f(v), 1.f + hi2f(v)), vv = (w >> 1) == 2 ? v1 : v;
    if (lane < 16) *lds<f2>(L_SC + (u32)w * 128u + lane * 8) = (f2){lo2f(vv), hi2f(vv)};
  }
  BAR();
  const int c8 = lane & 7;
  float ga[8], wp[8];
  {
    const f4 g0 = *lds<f4>(L_SC + c8 * 32), g1 = *lds<f4>(L_SC + c8 * 32 + 16), s0 = *lds<f4>(L_SC + 256 + c8 * 32), s1 = *lds<f4>(L_SC + 256 + c8 * 32 + 16);
    const f4 c0 = *lds<f4>(L_SC + 512 + c8 * 32), c1 = *lds<f4>(L_SC + 512 + c8 * 32 + 16);
    float bs = 0.f, cs = 0.f;
#pragma unroll
    for (int e = 0; e < 8; ++e) {
      const float wr = (e & 1) ? hi2f(wrv[e >> 1]) : lo2f(wrv[e >> 1]);
      ga[e] = e < 4 ? g0[e & 3] : g1[e & 3]; wp[e] = (e < 4 ? c0[e & 3] : c1[e & 3]) * wr;
      bs += wp[e]; cs += (e < 4 ? s0[e & 3] : s1[e & 3]) * wr;
    }
    bs = sum8(bs); cs = sum8(cs);
    if (w == 0) sm4c<0>(0xFull, (u32)lane * 16u, (v4u){__float_as_uint(bs), tag, __float_as_uint(cs), tag}, WS + WS_BC + (u32)(wt * 8 + sl) * 128u);
  }
  v4u og[8]; v2u ocs;
  const u64 ob = WS + WS_O + (u32)(wt * 2 + om) * O_TS + (u32)kq * 8192u + 4096u;
  for (int it = 0; it < SPIN_CAP; ++it) {
    ld4c<-4096>(og[0], l16, ob); ld4c<-3072>(og[1], l16, ob); ld4c<-2048>(og[2], l16, ob); ld4c<-1024>(og[3], l16, ob);
    ld4c<0>(og[4], l16, ob); ld4c<1024>(og[5], l16, ob); ld4c<2048>(og[6], l16, ob); ld4c<3072>(og[7], l16, ob);
    ld2c<0>(ocs, (u32)c8 * 128u, WS + WS_OCS + (u32)((wt * 2 + om) * 32 + kq * 8) * 128u);
    asm volatile("s_waitcnt vmcnt(0)" : "+v"(og[0]), "+v"(og[1]), "+v"(og[2]), "+v"(og[3]), "+v"(og[4]), "+v"(og[5]), "+v"(og[6]), "+v"(og[7]), "+v"(ocs));
    NP(3);
    const u32 t = x32(x16(rowsum(sum4(og[0]) + sum4(og[1]) + sum4(og[2]) + sum4(og[3]) + sum4(og[4]) + sum4(og[5]) + sum4(og[6]) + sum4(og[7]) - (lane < 8 ? ocs.x : 0u))));
    if (__all(t == 0u && ocs.y == tag)) break;
  }
  STAMP(15);
  const int fpair = xcd + 8 * (bi >> 1), fs = fpair * 2 + (bi & 1);
  const bool moe = bi < 8 && fpair < 28, fin = bi >= 8 && bi < 16;
  v4u bcg; ld4c<0>(bcg, (u32)c8 * 128u, WS + WS_BC + (u32)wt * 1024u);
  if (moe) {
    const u32 go = (u32)(lane >> 2) * 3584u + (u32)(lane & 3) * 16u, dvo = (u32)(lane >> 3) * 1024u + (u32)(lane & 7) * 16u;
    for (int n = 0; n < 4; ++n) {
      dma(go, (u64)gwg + (u32)(w * 64 + n * 16) * 3584u + (u32)fs * 64u, L_R0 + (u32)w * 8192u + (u32)n * 1024u);
      dma(go, (u64)gwu + (u32)(w * 64 + n * 16) * 3584u + (u32)fs * 64u, L_R0 + (u32)w * 8192u + 4096u + (u32)n * 1024u);
    }
    (void)dvo;
  }
  {
    f16v a0 = {}, a1 = {};
#pragma unroll
    for (int s = 0; s < 8; ++s) { a0 = mfma32(bw[s][0], og[s], a0); a1 = mfma32(bw[s][1], og[s], a1); }
    const u32 po = L_R1 + (u32)(kq * 64 + om * 32 + (lane & 31)) * 272u + (u32)h * 16u;
#pragma unroll
    for (int g = 0; g < 4; ++g) {
      *lds<f4>(po + g * 32) = (f4){a0[4 * g], a0[4 * g + 1], a0[4 * g + 2], a0[4 * g + 3]};
      *lds<f4>(po + 128 + g * 32) = (f4){a1[4 * g], a1[4 * g + 1], a1[4 * g + 2], a1[4 * g + 3]};
    }
  }
  BAR();
  float x1[8];
  {
    const u32 po = L_R1 + (u32)(tid >> 3) * 272u + (u32)c8 * 32u;
    const f4 u0 = (*lds<f4>(po) + *lds<f4>(po + 17408)) + (*lds<f4>(po + 34816) + *lds<f4>(po + 52224));
    const f4 u1 = (*lds<f4>(po + 16) + *lds<f4>(po + 17424)) + (*lds<f4>(po + 34832) + *lds<f4>(po + 52240));
    float a = 0.f, q = 0.f, d = 0.f;
#pragma unroll
    for (int e = 0; e < 8; ++e) {
      x1[e] = ((e & 1) ? hi2f(xres[e >> 1]) : lo2f(xres[e >> 1])) + ga[e] * (e < 4 ? u0[e & 3] : u1[e & 3]);
      a += x1[e]; q += x1[e] * x1[e]; d += x1[e] * wp[e];
    }
    const u32 ua = __float_as_uint(sum8(a)), uq = __float_as_uint(sum8(q)), ud = __float_as_uint(sum8(d));
    sm4c<0>(0x0101010101010101ull, (u32)(lane >> 3) * 16u, (v4u){ua, uq, ud, ua + uq + ud + tag}, WS + WS_ST + (u32)((wt * 8 + w) * 8 + sl) * 128u);
  }
  STAMP(16);
  float h2[8], base[8], gb[8], rr;
  {
    v4u sg;
    for (int it = 0; it < SPIN_CAP; ++it) {
      ld4c<0>(sg, (u32)c8 * 128u + (u32)(lane >> 3) * 16u, WS + WS_ST + (u32)(wt * 8 + w) * 1024u);
      asm volatile("s_waitcnt vmcnt(0)" : "+v"(sg), "+v"(bcg));
      NP(4);
      if (__all(sg.x + sg.y + sg.z + tag == sg.w)) break;
    }
    for (int it = 0; it < SPIN_CAP; ++it) {
      if (__all(bcg.y == tag && bcg.w == tag)) break;
      ld4c<0>(bcg, (u32)c8 * 128u, WS + WS_BC + (u32)wt * 1024u); asm volatile("s_waitcnt vmcnt(0)" : "+v"(bcg));
    }
    const float mu = sum8(__uint_as_float(sg.x)) * (1.f / 512.f), var = fmaxf(sum8(__uint_as_float(sg.y)) * (1.f / 512.f) - mu * mu, 0.f);
    const float rs = __builtin_amdgcn_rsqf(var + 1e-5f);
    rr = rs * (sum8(__uint_as_float(sg.z)) - mu * sum8(__uint_as_float(bcg.x))) + sum8(__uint_as_float(bcg.z));
    if (c8 == 0) *lds<float>(L_RR + (u32)(tid >> 3) * 4u) = rr;
    const f4 s0 = *lds<f4>(L_SC + 256 + c8 * 32), s1 = *lds<f4>(L_SC + 256 + c8 * 32 + 16), c0 = *lds<f4>(L_SC + 512 + c8 * 32), c1 = *lds<f4>(L_SC + 512 + c8 * 32 + 16);
    const f4 g0 = *lds<f4>(L_SC + 768 + c8 * 32), g1 = *lds<f4>(L_SC + 768 + c8 * 32 + 16);
#pragma unroll
    for (int e = 0; e < 8; ++e) {
      h2[e] = (x1[e] - mu) * rs * (e < 4 ? c0[e & 3] : c1[e & 3]) + (e < 4 ? s0[e & 3] : s1[e & 3]);
      gb[e] = e < 4 ? g0[e & 3] : g1[e & 3]; base[e] = x1[e] + gb[e] * h2[e];
    }
  }
  STAMP(17);
  BAR();
  {
    const float rl = *lds<float>(L_RR + lane * 4);
    int i1, i2; top2(rl, lane, i1, i2);
    const int row = tid >> 3; const u64 cand = __ballot(row == i1 || row == i2); const u32 cj = row == i2 ? 1u : 0u;
    if (cand) {
      const u32 o = cj * 2048u + (u32)c8 * 16u; const u64 hb = WS + WS_H2C + (u32)wt * 4096u + (u32)sl * 256u;
      sm4c<0>(cand, o, (v4u){pk(h2[0], h2[1]), tag, pk(h2[2], h2[3]), tag}, hb); sm4c<128>(cand, o, (v4u){pk(h2[4], h2[5]), tag, pk(h2[6], h2[7]), tag}, hb);
    }
    if (w == 0) {
      const int ii = (lane & 1) ? i2 : i1; const float rv = *lds<float>(L_RR + ii * 4);
      sm4c<0>(0xFull, l16, (v4u){__float_as_uint(rv), tag, (u32)(wt * 64 + ii), tag}, WS + WS_CAND + (u32)(sl * 32 + wt) * 128u);
    }
    STAMP(18);
    st4c<0>((u32)row * 1024u + (u32)c8 * 16u, (v4u){pk(base[0], base[1]), pk(base[2], base[3]), pk(base[4], base[5]), pk(base[6], base[7])}, OUT + (u32)wt * 65536u + (u32)sl * 128u);
    if (moe) {
      const u32 dvo = (u32)(lane >> 3) * 1024u + (u32)(lane & 7) * 16u;
      for (int n = 0; n < 4; ++n) dma(dvo, (u64)gwd + (u32)(fs * 32 + n * 8) * 1024u + (u32)w * 128u, L_R1 + (u32)w * 4096u + (u32)n * 1024u);
    }
    if (cand) {
      vmw<0>();
      {
        const float sg = sigm(rr); const u32 o = cj * 8192u + (u32)c8 * 16u; const u64 bb = WS + WS_BASE + (u32)wt * 16384u + (u32)sl * 1024u;
#define STB(e) sm4c<e * 128>(cand, o, (v4u){__float_as_uint(base[e]), tag, __float_as_uint(gb[e] * sg), tag}, bb);
        STB(0) STB(1) STB(2) STB(3) STB(4) STB(5) STB(6) STB(7)
      }
    }
  }
  if (moe || (fin && w == 0)) {
    v4u wg[2][4], wd[4];
    const u32 g = (u32)lane >> 4;
    if (moe) {
      vmw<0>();
#pragma unroll
      for (int s = 0; s < 2; ++s)
#pragma unroll
        for (int c = 0; c < 4; ++c) { const u32 a = L_R0 + (u32)w * 8192u + (c >> 1) * 4096 + (s * 32 + g * 8 + trq) * 64u + (c & 1) * 32 + (u32)(lane & 3) * 8u; wg[s][c] = trd2(a, a + 256); }
#pragma unroll
      for (int c = 0; c < 4; ++c) { const u32 a = L_R1 + (u32)w * 4096u + (g * 8 + trq) * 128u + c * 32 + (u32)(lane & 3) * 8u; wd[c] = trd2(a, a + 512); }
    }
    v4u cg;
    for (int it = 0; it < SPIN_CAP; ++it) {
      ld4c<0>(cg, (u32)(lane >> 1) * 128u + (u32)(lane & 1) * 16u, WS + WS_CAND + (u32)xcd * 4096u);
      asm volatile("s_waitcnt vmcnt(0)" : "+v"(cg));
      NP(5);
      if (__all(cg.y == tag && cg.w == tag)) break;
    }
    STAMP(19);
    int j1, j2; top2(__uint_as_float(cg.x), lane, j1, j2);
    if (moe) {
      const u32 jj = (u32)((lane & 1) ? j2 : j1), ho = jj * 2048u + g * 16u;
      v4u hg[4];
      for (int it = 0; it < SPIN_CAP; ++it) {
        const u64 hb = WS + WS_H2C + (u32)w * 256u;
        ld4c<0>(hg[0], ho, hb); ld4c<128>(hg[1], ho, hb); ld4c<64>(hg[2], ho, hb); ld4c<192>(hg[3], ho, hb);
        asm volatile("s_waitcnt vmcnt(0)" : "+v"(hg[0]), "+v"(hg[1]), "+v"(hg[2]), "+v"(hg[3]));
        const u32 bad = (hg[0].y ^ tag) | (hg[0].w ^ tag) | (hg[1].y ^ tag) | (hg[1].w ^ tag) | (hg[2].y ^ tag) | (hg[2].w ^ tag) | (hg[3].y ^ tag) | (hg[3].w ^ tag);
        if (__all(bad == 0)) break;
      }
      STAMP(20);
      const v4u a0 = {hg[0].x, hg[0].z, hg[1].x, hg[1].z}, a1 = {hg[2].x, hg[2].z, hg[3].x, hg[3].z};
      f4 c[4];
#pragma unroll
      for (int t = 0; t < 4; ++t) c[t] = mfma16(a1, wg[1][t], mfma16(a0, wg[0][t], (f4){}));
      const f4 cc = g & 2 ? (g & 1 ? c[3] : c[2]) : (g & 1 ? c[1] : c[0]);
      const u32 ro = L_KO + 4096u + ((g & 1) * 16u + (u32)(lane & 15)) * 80u + (g >> 1) * 32u + (u32)w * 4u;
      *lds<float>(ro) = cc.x; *lds<float>(ro + 2560) = cc.y;
      BAR();
      const u32 rd = L_KO + 4096u + (u32)lane * 80u;
      const f4 g0 = *lds<f4>(rd) + *lds<f4>(rd + 16), u0 = *lds<f4>(rd + 32) + *lds<f4>(rd + 48);
      const float gt = (g0.x + g0.y) + (g0.z + g0.w), up = (u0.x + u0.y) + (u0.z + u0.w);
      *lds<u16>(L_ACT + (u32)w * 128u + lane * 2) = (u16)pk(gt * sigm(gt) * up, 0.f);
      asm volatile("s_waitcnt lgkmcnt(0)" ::: "memory");
      const v4u af = *lds<v4u>(L_ACT + (u32)w * 128u + (u32)(lane & 1) * 64u + g * 16u);
#pragma unroll
      for (int t = 0; t < 4; ++t) c[t] = mfma16(af, wd[t], (f4){});
      const f4 yy = g & 2 ? (g & 1 ? c[3] : c[2]) : (g & 1 ? c[1] : c[0]);
      const u64 yb = WS + WS_YP + (u32)fs * 8192u + (u32)w * 512u + 4096u;
      st2c<-4096>((u32)lane * 8u, (v2u){__float_as_uint(yy.x), tag}, yb); st2c<0>((u32)lane * 8u, (v2u){__float_as_uint(yy.y), tag}, yb);
      STAMP(21);
    } else {
      const int f = (bi - 8) * 8 + xcd, tk = f >> 5, cb = f & 31;
      if (f == 0) sm1c<WS_EPOCH>(1ull, 0u, tag, WS);
      const int jj = tk ? j2 : j1;
      const u32 token = (u32)__builtin_amdgcn_readlane((int)cg.z, jj) & 2047u;
      const u32 col = (u32)(cb & 3) * 16u + (u32)c8 * 2u, bo = (u32)jj * 8192u + (u32)(cb >> 2) * 1024u + (col & 7u) * 128u + (col >> 3) * 16u;
      v4u b0, b1, y[7];
      const u32 yo = (u32)(lane >> 3) * 8192u + (u32)c8 * 16u; const u64 yb = WS + WS_YP + (u32)tk * 4096u + (u32)cb * 128u;
      for (int it = 0; it < SPIN_CAP; ++it) {
        ld4c<0>(b0, bo, WS + WS_BASE); ld4c<128>(b1, bo, WS + WS_BASE);
        asm volatile("s_waitcnt vmcnt(0)" : "+v"(b0), "+v"(b1));
        if (__all(((b0.y ^ tag) | (b0.w ^ tag) | (b1.y ^ tag) | (b1.w ^ tag)) == 0)) break;
      }
      for (int it = 0; it < SPIN_CAP; ++it) {
#pragma unroll
        for (int u = 0; u < 7; ++u) ld4c<0>(y[u], yo, yb + u * 65536u);
        asm volatile("s_waitcnt vmcnt(0)" : "+v"(y[0]), "+v"(y[1]), "+v"(y[2]), "+v"(y[3]), "+v"(y[4]), "+v"(y[5]), "+v"(y[6]));
        u32 bad = 0;
#pragma unroll
        for (int u = 0; u < 7; ++u) bad |= (y[u].y ^ tag) | (y[u].w ^ tag);
        if (__all(bad == 0)) break;
      }
      STAMP(20);
      float y0 = 0.f, y1 = 0.f;
#pragma unroll
      for (int u = 0; u < 7; ++u) { y0 += __uint_as_float(y[u].x); y1 += __uint_as_float(y[u].z); }
      y0 += dppf<ROR8>(y0); y1 += dppf<ROR8>(y1); y0 = x32f(x16f(y0)); y1 = x32f(x16f(y1));
      const u32 res = pk(__uint_as_float(b0.x) + __uint_as_float(b0.z) * y0, __uint_as_float(b1.x) + __uint_as_float(b1.z) * y1);
      sm1c<0>(0xFFull, token * 1024u + (u32)cb * 32u + (u32)lane * 4u, res, OUT);
      STAMP(21);
    }
  }
  asm volatile("KERNEL_END_MARK:");
}

extern "C" void kernel_launch(void* const* d_in, const int* in_sizes, int n_in, void* d_out, int out_size, void* d_ws, size_t ws_size,
                              hipStream_t stream) {
  fused_kernel<<<256, 512, 0, stream>>>((const char*)d_in[0], (const char*)d_in[1], (const char*)d_in[2], (const char*)d_in[4], (const char*)d_in[5],
                                        (const char*)d_in[6], (const char*)d_in[7], (const char*)d_in[8], (const char*)d_in[9], (char*)d_out, (char*)d_ws);
}
```

```cpp
#include <hip/hip_runtime.h>
#include <hip/hip_bf16.h>
constexpr unsigned WS_DIAG = 0x4000000;
__global__ void __launch_bounds__(512, 2)
fused_kernel(const void* x, const void* wqkv, const void* wmod, const void* c, void* ws, const void* wo, const void* wr, void* out,
             const void* wg, const void* wu, const void* wd) {
  __shared__ __attribute__((aligned(16))) char smem[163840];
  asm volatile("" :: "s"((__attribute__((address_space(3))) char*)smem));
  asm volatile(
    ".Lkstart:\n"
    "s_load_dwordx16 s[8:23], s[0:1], 0x0\n"
    "s_load_dwordx4 s[24:27], s[0:1], 0x40\n"
    "s_load_dwordx2 s[28:29], s[0:1], 0x50\n"
    "s_getpc_b64 s[54:55]\n"
    "v_readfirstlane_b32 s30, v0\n"
    "v_and_b32 v1, 63, v0\n"
    "s_andn2_b32 s56, s2, 7\n"
    "s_lshr_b32 s30, s30, 6\n"
    "s_add_u32 s56, s56, s30\n"
    "s_lshl_b32 s56, s56, 7\n"
    "s_min_u32 s56, s56, .Lkend-.Lkstart-64\n"
    "v_mov_b32 v3, s56\n"
    "global_load_dword v4, v3, s[54:55]\n"
    "s_and_b32 s31, s2, 7\n"
    "s_lshr_b32 s32, s2, 3\n"
    "s_and_b32 s39, s30, 3\n"
    "v_lshlrev_b32 v2, 4, v1\n"
    "s_lshr_b32 s56, s32, 2\n"
    "s_and_b32 s57, s31, 6\n"
    "s_lshl2_add_u32 s33, s57, s56\n"
    "s_add_u32 s35, s32, s56\n"
    "s_and_b32 s35, s35, 3\n"
    "s_and_b32 s57, s31, 1\n"
    "s_lshl1_add_u32 s64, s32, s57\n"
    "s_lshr_b32 s65, s31, 1\n"
    "s_lshl2_add_u32 s56, s65, s39\n"
    "s_lshl_b32 s56, s56, 5\n"
    "v_lshrrev_b32 v238, 3, v1\n"
    "v_lshl_add_u32 v238, v238, 2, s56\n"
    "v_and_b32 v239, 3, v1\n"
    "v_add_lshl_u32 v162, v238, v239, 1\n"
    "s_lshl_b32 s57, s64, 7\n"
    "s_add_u32 s57, s57, 3072\n"
    "v_mul_u32_u24 v238, 6144, v238\n"
    "v_and_b32 v239, 0x70, v2\n"
    "v_add3_u32 v160, v238, v239, s57\n"
    "v_add_u32 v161, 12288, v160\n"
    "s_lshr_b32 s59, s32, 3\n"
    "s_sub_u32 s59, s59, 1\n"
    "s_lshr_b32 s60, s59, 1\n"
    "s_xor_b32 s61, s30, 4\n"
    "s_or_b32 s60, s60, s61\n"
    "s_lshl1_add_u32 s59, s65, s59\n"
    "v_lshl_add_u32 v242, s59, 6, v1\n"
    "v_mul_u32_u24 v242, 6144, v242\n"
    "s_and_b32 s59, s64, 15\n"
    "v_lshl_add_u32 v242, s59, 7, v242\n"
    "s_lshr_b32 s66, s2, 6\n"
    "s_lshr_b32 s56, s30, 2\n"
    "s_or_b32 s66, s66, s56\n"
    "s_waitcnt lgkmcnt(0)\n"
    "s_cbranch_scc1 .Lnp0\n"
    "global_load_dwordx4 v[88:91], v160, s[12:13] offset:-3072\n"
    "global_load_dwordx4 v[92:95], v160, s[12:13] offset:3072\n"
    "global_load_dwordx4 v[96:99], v161, s[12:13] offset:-3072\n"
    "global_load_dwordx4 v[100:103], v161, s[12:13] offset:3072\n"
    "global_load_ushort v104, v162, s[14:15]\n"
    ".Lnp0:\n"
    "s_cmp_lg_u32 s60, 0\n"
    "s_cbranch_scc1 .Lnh0\n"
    "global_load_dword v105, v242, s[12:13]\n"
    ".Lnh0:\n"
    "s_load_dword s38, s[16:17], 0x0\n"
    "s_and_b32 s57, s31, 1\n"
    "s_and_b32 s56, s32, 3\n"
    "s_lshl2_add_u32 s34, s57, s56\n"
    "s_add_u32 s54, s35, 0\n"
    "s_and_b32 s54, s54, 3\n"
    "s_mul_i32 s54, s54, 393216\n"
    "s_add_u32 s40, s54, 1024\n"
    "s_add_u32 s54, s35, 1\n"
    "s_and_b32 s54, s54, 3\n"
    "s_mul_i32 s54, s54, 393216\n"
    "s_add_u32 s41, s54, 1024\n"
    "s_add_u32 s54, s35, 2\n"
    "s_and_b32 s54, s54, 3\n"
    "s_mul_i32 s54, s54, 393216\n"
    "s_add_u32 s42, s54, 1024\n"
    "s_add_u32 s54, s35, 3\n"
    "s_and_b32 s54, s54, 3\n"
    "s_mul_i32 s54, s54, 393216\n"
    "s_add_u32 s43, s54, 1024\n"
    "s_lshr_b32 s55, s32, 3\n"
    "s_and_b32 s55, s55, 2\n"
    "s_lshr_b32 s56, s30, 2\n"
    "s_or_b32 s55, s55, s56\n"
    "v_lshrrev_b32 v238, 3, v1\n"
    "v_lshrrev_b32 v239, 2, v1\n"
    "v_and_b32 v239, 4, v239\n"
    "v_and_b32 v240, 7, v1\n"
    "v_xor_b32 v239, v239, v240\n"
    "v_lshlrev_b32 v239, 4, v239\n"
    "v_mul_u32_u24 v238, 3072, v238\n"
    "s_lshl_b32 s57, s34, 7\n"
    "v_add3_u32 v238, v238, v239, s57\n"
    "s_xor_b32 s56, s55, 0\n"
    "s_lshl2_add_u32 s56, s56, s39\n"
    "s_lshl_b32 s44, s56, 10\n"
    "s_mul_i32 s56, s56, 24576\n"
    "v_add_u32 v6, s56, v238\n"
    "s_xor_b32 s56, s55, 1\n"
    "s_lshl2_add_u32 s56, s56, s39\n"
    "s_lshl_b32 s45, s56, 10\n"
    "s_mul_i32 s56, s56, 24576\n"
    "v_add_u32 v7, s56, v238\n"
    "s_xor_b32 s56, s55, 2\n"
    "s_lshl2_add_u32 s56, s56, s39\n"
    "s_lshl_b32 s46, s56, 10\n"
    "s_mul_i32 s56, s56, 24576\n"
    "v_add_u32 v8, s56, v238\n"
    "s_xor_b32 s56, s55, 3\n"
    "s_lshl2_add_u32 s56, s56, s39\n"
    "s_lshl_b32 s47, s56, 10\n"
    "s_mul_i32 s56, s56, 24576\n"
    "v_add_u32 v9, s56, v238\n"
    "s_lshl3_add_u32 s54, s33, s30\n"
    "s_lshl_b32 s54, s54, 13\n"
    "s_and_b32 s55, s32, 3\n"
    "s_xor_b32 s56, s55, 0\n"
    "s_lshl_b32 s56, s56, 11\n"
    "s_add_u32 s56, s56, s54\n"
    "v_add_u32 v114, s56, v2\n"
    "s_xor_b32 s56, s55, 1\n"
    "s_lshl_b32 s56, s56, 11\n"
    "s_add_u32 s56, s56, s54\n"
    "v_add_u32 v115, s56, v2\n"
    "s_xor_b32 s56, s55, 2\n"
    "s_lshl_b32 s56, s56, 11\n"
    "s_add_u32 s56, s56, s54\n"
    "v_add_u32 v116, s56, v2\n"
    "s_xor_b32 s56, s55, 3\n"
    "s_lshl_b32 s56, s56, 11\n"
    "s_add_u32 s56, s56, s54\n"
    "v_add_u32 v117, s56, v2\n"
    "s_sleep 2\n"
    "global_load_dwordx4 v[56:59], v114, s[8:9] offset:0\n"
    "global_load_dwordx4 v[60:63], v114, s[8:9] offset:1024\n"
    "s_add_u32 m0, s44, 0\n"
    "s_add_u32 s62, s10, s40\n"
    "s_addc_u32 s63, s11, 0\n"
    "global_load_lds_dwordx4 v6, s[62:63]\n"
    "s_mov_b32 s49, 0x3f803f80\n"
    "s_mov_b32 s50, 0xffff0000\n"
    "s_movk_i32 s48, 0x4000\n"
    "s_mov_b64 s[52:53], -1\n"
    "s_lshr_b32 s56, s32, 3\n"
    "s_lshl2_add_u32 s36, s31, s56\n"
    "s_and_b32 s37, s32, 7\n"
    "s_sub_u32 s99, s2, 64\n"
    "s_cmp_lt_u32 s99, 128\n"
    "s_cselect_b32 s99, 1, 0\n"
    "s_mov_b32 s98, 0x3b000000\n"
    "s_lshr_b32 s56, s30, 2\n"
    "s_mul_i32 s56, s56, 8448\n"
    "s_lshl_b32 s57, s39, 7\n"
    "s_add_u32 s56, s56, s57\n"
    "s_add_u32 s56, s56, 65536\n"
    "v_lshrrev_b32 v238, 4, v1\n"
    "v_subrev_u32 v238, s35, v238\n"
    "v_and_b32 v238, 3, v238\n"
    "v_mul_u32_u24 v238, 16896, v238\n"
    "v_and_b32 v239, 15, v1\n"
    "v_mul_u32_u24 v239, 528, v239\n"
    "v_add3_u32 v238, v238, v239, s56\n"
    "v_add_u32 v228, 141312, v2\n"
    "s_and_b32 s55, s32, 3\n"
    "s_xor_b32 s56, s55, 0\n"
    "v_lshl_add_u32 v224, s56, 5, v238\n"
    "s_xor_b32 s56, s55, 1\n"
    "v_lshl_add_u32 v225, s56, 5, v238\n"
    "s_xor_b32 s56, s55, 2\n"
    "v_lshl_add_u32 v226, s56, 5, v238\n"
    "s_xor_b32 s56, s55, 3\n"
    "v_lshl_add_u32 v227, s56, 5, v238\n"
    "v_mov_b64 v[120:121], 0\n"
    "v_mov_b64 v[122:123], 0\n"
    "v_mov_b64 v[124:125], 0\n"
    "v_mov_b64 v[126:127], 0\n"
    "v_mov_b64 v[128:129], 0\n"
    "v_mov_b64 v[130:131], 0\n"
    "v_mov_b64 v[132:133], 0\n"
    "v_mov_b64 v[134:135], 0\n"
    "v_lshlrev_b32 v230, 4, v0\n"
    "v_add_u32 v230, 4096, v230\n"
    "v_bfe_u32 v241, v0, 2, 6\n"
    "v_lshlrev_b32 v232, 4, v241\n"
    "v_bfe_u32 v241, v1, 1, 1\n"
    "v_lshl_add_u32 v232, v241, 10, v232\n"
    "v_and_b32 v241, 1, v1\n"
    "v_lshl_add_u32 v232, v241, 3, v232\n"
    "v_add_u32 v232, 141312, v232\n"
    "s_cmp_ge_u32 s30, 4\n"
    "s_cbranch_scc1 .LM1\n"
    "v_mov_b32 v106, 0\n"
    "v_mov_b32 v107, 0\n"
    "v_mov_b32 v108, 0\n"
    "v_mov_b32 v109, 0\n"
    "v_mov_b32 v110, 0\n"
    "v_mov_b32 v111, 0\n"
    "v_mov_b32 v112, 0\n"
    "v_mov_b32 v113, 0\n"
    "v_and_b32 v238, 7, v1\n"
    "v_lshrrev_b32 v239, 3, v1\n"
    "v_lshl_add_u32 v238, v238, 3, v239\n"
    "s_lshl_b32 s56, s39, 8\n"
    "s_add_u32 s56, s56, 146432\n"
    "v_lshl_add_u32 v163, v238, 2, s56\n"
    "s_mul_i32 s56, s65, 24576\n"
    "s_lshl_b32 s57, s64, 9\n"
    "s_add_u32 s56, s56, s57\n"
    "s_add_u32 s56, s56, 4096\n"
    "v_lshl_add_u32 v164, v1, 3, s56\n"
    "v_lshlrev_b32 v165, 2, v1\n"
    "v_add_u32 v165, 146432, v165\n"
    "v_lshrrev_b32 v139, 2, v1\n"
    "v_lshl_add_u32 v139, s32, 4, v139\n"
    "v_mul_u32_u24 v139, 3072, v139\n"
    "v_and_b32 v239, 3, v1\n"
    "v_lshl_add_u32 v139, v239, 7, v139\n"
    "s_and_b32 s57, s31, 1\n"
    "s_lshl_b32 s57, s57, 9\n"
    "s_cmp_eq_u32 s39, 0\n"
    "s_cselect_b32 s58, 2048, 0\n"
    "s_add_u32 s57, s57, s58\n"
    "v_add_u32 v139, s57, v139\n"
    "v_and_b32 v239, 7, v1\n"
    "v_mul_u32_u24 v240, 592128, v239\n"
    "v_add_u32 v240, 2686976, v240\n"
    "v_mul_u32_u24 v239, 297216, v239\n"
    "v_add_u32 v239, 7372800, v239\n"
    "v_cmp_gt_u32 vcc, 8, v1\n"
    "s_mov_b32 s58, 2359296\n"
    "s_nop 0\n"
    "v_cndmask_b32 v239, v239, v240, vcc\n"
    "v_writelane_b32 v239, s58, 16\n"
    "s_cmp_lt_u32 s39, 2\n"
    "s_cselect_b64 vcc, -1, 0\n"
    "s_cselect_b64 s[60:61], s[10:11], s[16:17]\n"
    "v_cndmask_b32 v139, v239, v139, vcc\n"
    "s_lshl2_add_u32 s57, s34, s39\n"
    "s_lshl_b32 s56, s33, 1\n"
    "v_and_b32 v241, 3, v1\n"
    "v_lshlrev_b32 v241, 2, v241\n"
    "s_mov_b32 s54, 0x4320\n"
    "v_bfe_u32 v241, s54, v241, 4\n"
    "s_sub_i32 s56, s56, 8\n"
    "v_add_u32 v241, s56, v241\n"
    "v_cmp_gt_i32 vcc, 0, v241\n"
    "s_and_b32 s51, vcc_lo, 15\n"
    "s_lshl_b32 s51, s51, 1\n"
    "v_max_i32 v241, 0, v241\n"
    "s_lshr_b32 s54, s57, 1\n"
    "s_lshl_b32 s54, s54, 8\n"
    "s_and_b32 s57, s57, 1\n"
    "s_lshl3_add_u32 s54, s57, s54\n"
    "s_add_u32 s54, s54, 327680\n"
    "v_lshl_add_u32 v15, v241, 12, s54\n"
    "v_lshrrev_b32 v241, 4, v1\n"
    "v_lshl_add_u32 v15, v241, 5, v15\n"
    "s_cmp_lg_u32 s66, 0\n"
    "s_cbranch_scc1 .Lnp1\n"
    "s_waitcnt vmcnt(3)\n"
    "v_lshlrev_b32 v104, 16, v104\n"
    "s_mov_b32 s58, 0xff00ff00\n"
    "s_mov_b32 s59, 0xff00ff00\n"
    "v_mul_f32 v238, 0xbfb8aa3b, v104\n"
    "v_exp_f32 v238, v238\n"
    "s_nop 0\n"
    "v_add_f32 v238, 1.0, v238\n"
    "v_rcp_f32 v238, v238\n"
    "s_nop 0\n"
    "v_mul_f32 v238, v238, v104\n"
    "v_cvt_pk_bf16_f32 v104, v238, 0\n"
    "s_nop 1\n"
    "v_mov_b32_dpp v238, v104 quad_perm:[0,0,0,0] row_mask:0xf bank_mask:0xf bound_ctrl:1\n"
    "v_mov_b32_dpp v239, v104 quad_perm:[1,1,1,1] row_mask:0xf bank_mask:0xf bound_ctrl:1\n"
    "v_mov_b32_dpp v240, v104 quad_perm:[2,2,2,2] row_mask:0xf bank_mask:0xf bound_ctrl:1\n"
    "v_mov_b32_dpp v241, v104 quad_perm:[3,3,3,3] row_mask:0xf bank_mask:0xf bound_ctrl:1\n"
    "v_lshlrev_b32 v242, 16, v238\n"
    "v_lshlrev_b32 v243, 16, v239\n"
    "v_lshlrev_b32 v244, 16, v240\n"
    "v_lshlrev_b32 v245, 16, v241\n"
    "v_dot2c_f32_bf16 v106, v88, v238\n"
    "v_dot2c_f32_bf16 v107, v88, v242\n"
    "v_dot2c_f32_bf16 v108, v89, v238\n"
    "v_dot2c_f32_bf16 v109, v89, v242\n"
    "v_dot2c_f32_bf16 v110, v90, v238\n"
    "v_dot2c_f32_bf16 v111, v90, v242\n"
    "v_dot2c_f32_bf16 v112, v91, v238\n"
    "v_dot2c_f32_bf16 v113, v91, v242\n"
    "v_dot2c_f32_bf16 v106, v92, v239\n"
    "v_dot2c_f32_bf16 v107, v92, v243\n"
    "v_dot2c_f32_bf16 v108, v93, v239\n"
    "v_dot2c_f32_bf16 v109, v93, v243\n"
    "v_dot2c_f32_bf16 v110, v94, v239\n"
    "v_dot2c_f32_bf16 v111, v94, v243\n"
    "v_dot2c_f32_bf16 v112, v95, v239\n"
    "v_dot2c_f32_bf16 v113, v95, v243\n"
    "v_dot2c_f32_bf16 v106, v96, v240\n"
    "v_dot2c_f32_bf16 v107, v96, v244\n"
    "v_dot2c_f32_bf16 v108, v97, v240\n"
    "v_dot2c_f32_bf16 v109, v97, v244\n"
    "v_dot2c_f32_bf16 v110, v98, v240\n"
    "v_dot2c_f32_bf16 v111, v98, v244\n"
    "v_dot2c_f32_bf16 v112, v99, v240\n"
    "v_dot2c_f32_bf16 v113, v99, v244\n"
    "v_dot2c_f32_bf16 v106, v100, v241\n"
    "v_dot2c_f32_bf16 v107, v100, v245\n"
    "v_dot2c_f32_bf16 v108, v101, v241\n"
    "v_dot2c_f32_bf16 v109, v101, v245\n"
    "v_dot2c_f32_bf16 v110, v102, v241\n"
    "v_dot2c_f32_bf16 v111, v102, v245\n"
    "v_dot2c_f32_bf16 v112, v103, v241\n"
    "v_dot2c_f32_bf16 v113, v103, v245\n"
    "v_permlane32_swap_b32 v106, v110\n"
    "v_permlane32_swap_b32 v107, v111\n"
    "v_permlane32_swap_b32 v108, v112\n"
    "v_permlane32_swap_b32 v109, v113\n"
    "v_add_f32 v106, v106, v110\n"
    "v_add_f32 v107, v107, v111\n"
    "v_add_f32 v108, v108, v112\n"
    "v_add_f32 v109, v109, v113\n"
    "s_nop 0\n"
    "v_permlane16_swap_b32 v106, v108\n"
    "v_permlane16_swap_b32 v107, v109\n"
    "v_add_f32 v106, v106, v108\n"
    "v_add_f32 v107, v107, v109\n"
    "s_nop 0\n"
    "v_add_f32_dpp v106, v106, v106 row_ror:8 row_mask:0xf bank_mask:0xf bound_ctrl:1\n"
    "v_add_f32_dpp v107, v107, v107 row_ror:8 row_mask:0xf bank_mask:0xf bound_ctrl:1\n"
    "v_cndmask_b32_e64 v106, v106, v107, s[58:59]\n"
    "ds_write_b32 v163, v106\n"
    ".Lnp1:\n"
    "s_or_b32 s56, s66, s30\n"
    "s_waitcnt lgkmcnt(0)\n"
    "s_barrier\n"
    "s_add_u32 s38, s38, 1\n"
    "s_cmp_lg_u32 s56, 0\n"
    "s_cbranch_scc1 .Lnp2\n"
    "ds_read_b32 v106, v165\n"
    "ds_read_b32 v107, v165 offset:256\n"
    "ds_read_b32 v108, v165 offset:512\n"
    "ds_read_b32 v109, v165 offset:768\n"
    "v_mov_b32 v111, s38\n"
    "s_waitcnt lgkmcnt(0)\n"
    "v_add_f32 v106, v106, v107\n"
    "v_add_f32 v108, v108, v109\n"
    "v_add_f32 v110, v106, v108\n"
    "global_store_dwordx2 v164, v[110:111], s[16:17] sc1\n"
    ".Lnp2:\n"
    "s_barrier\n"
    "global_load_dwordx4 v[64:67], v115, s[8:9] offset:0\n"
    "global_load_dwordx4 v[68:71], v115, s[8:9] offset:1024\n"
    "global_load_dwordx4 v[72:75], v116, s[8:9] offset:0\n"
    "global_load_dwordx4 v[76:79], v116, s[8:9] offset:1024\n"
    "global_load_dwordx4 v[80:83], v117, s[8:9] offset:0\n"
    "global_load_dwordx4 v[84:87], v117, s[8:9] offset:1024\n"
    "s_add_u32 m0, s46, 0\n"
    "s_add_u32 s62, s10, s40\n"
    "s_addc_u32 s63, s11, 0\n"
    "global_load_lds_dwordx4 v8, s[62:63]\n"
    "v_lshlrev_b32 v10, 1, v1\n"
    "v_and_b32 v13, 0x70, v2\n"
    "v_lshrrev_b32 v243, 3, v1\n"
    "v_lshl_add_u32 v12, v243, 10, v13\n"
    "v_lshlrev_b32 v243, 2, v1\n"
    "v_and_b32 v243, 64, v243\n"
    "v_xor_b32 v11, v12, v243\n"
    "s_add_u32 m0, s47, 0\n"
    "s_nop 0\n"
    "global_load_lds_dwordx4 v9, s[62:63]\n"
    "v_lshrrev_b32 v243, 4, v1\n"
    "v_mul_u32_u24 v243, 24576, v243\n"
    "v_and_b32 v244, 0xf0, v2\n"
    "v_add_u32 v14, v243, v244\n"
    "v_lshl_add_u32 v233, s30, 13, v2\n"
    "v_xor_b32 v234, 0x8000, v233\n"
    "v_lshl_add_u32 v243, s30, 10, v2\n"
    "s_add_u32 m0, s44, 16384\n"
    "s_add_u32 s62, s10, s41\n"
    "s_addc_u32 s63, s11, 0\n"
    "global_load_lds_dwordx4 v6, s[62:63]\n"
    "v_add_u32 v235, 133120, v243\n"
    "v_xor_b32 v236, 4096, v243\n"
    "v_add_u32 v236, 133120, v236\n"
    "v_mov_b32 v244, 141312\n"
    "v_add_u32 v243, 155648, v243\n"
    "v_add_u32 v237, 3072, v233\n"
    "s_mov_b32 vcc_lo, 0xffff0000\n"
    "s_mov_b32 vcc_hi, 0xffff0000\n"
    "v_cndmask_b32 v249, v243, v244, vcc\n"
    "v_cndmask_b32 v237, v237, v244, vcc\n"
    "s_add_u32 m0, s45, 16384\n"
    "s_nop 0\n"
    "global_load_lds_dwordx4 v7, s[62:63]\n"
    "v_mov_b32 v250, v237\n"
    "s_lshl2_add_u32 s54, s32, s39\n"
    "v_bfe_u32 v229, v1, 2, 2\n"
    "v_lshl_add_u32 v229, s54, 2, v229\n"
    "v_mul_u32_u24 v229, 3584, v229\n"
    "s_min_u32 s54, s31, 6\n"
    "s_lshl_b32 s54, s54, 9\n"
    "s_add_u32 m0, s46, 16384\n"
    "s_nop 0\n"
    "global_load_lds_dwordx4 v8, s[62:63]\n"
    "v_and_b32 v243, 3, v1\n"
    "v_lshlrev_b32 v243, 7, v243\n"
    "v_add3_u32 v229, v229, v243, s54\n"
    "s_and_b32 s54, s37, 1\n"
    "s_lshl_b32 s54, s54, 2\n"
    "s_xor_b32 s54, s54, s30\n"
    "s_and_b32 s55, s54, 1\n"
    "s_add_u32 m0, s47, 16384\n"
    "s_nop 0\n"
    "global_load_lds_dwordx4 v9, s[62:63]\n"
    "s_lshr_b32 s56, s54, 1\n"
    "s_lshl_b32 s56, s56, 14\n"
    "s_add_u32 s56, s56, 65536\n"
    "v_bfe_u32 v238, v1, 2, 2\n"
    "v_lshlrev_b32 v3, 7, v238\n"
    "v_lshrrev_b32 v238, 1, v238\n"
    "v_lshl_add_u32 v3, v238, 6, v3\n"
    "s_add_u32 m0, s44, 32768\n"
    "s_add_u32 s62, s10, s42\n"
    "s_addc_u32 s63, s11, 0\n"
    "global_load_lds_dwordx4 v6, s[62:63]\n"
    "v_bfe_u32 v238, v1, 4, 1\n"
    "v_lshl_add_u32 v3, v238, 5, v3\n"
    "v_and_b32 v238, 3, v1\n"
    "v_lshl_add_u32 v3, v238, 3, v3\n"
    "v_lshrrev_b32 v238, 5, v1\n"
    "v_lshl_add_u32 v3, v238, 9, v3\n"
    "v_add_u32 v3, s56, v3\n"
    "s_add_u32 m0, s45, 32768\n"
    "s_nop 0\n"
    "global_load_lds_dwordx4 v7, s[62:63]\n"
    "s_lshl2_add_u32 s57, s34, s39\n"
    "s_lshl_b32 s56, s33, 1\n"
    "v_and_b32 v241, 3, v1\n"
    "v_lshlrev_b32 v241, 2, v241\n"
    "s_mov_b32 s54, 0x7651\n"
    "v_bfe_u32 v241, s54, v241, 4\n"
    "s_sub_i32 s56, s56, 8\n"
    "s_add_u32 m0, s46, 32768\n"
    "s_nop 0\n"
    "global_load_lds_dwordx4 v8, s[62:63]\n"
    "v_add_u32 v241, s56, v241\n"
    "v_max_i32 v241, 0, v241\n"
    "s_lshr_b32 s54, s57, 1\n"
    "s_lshl_b32 s54, s54, 8\n"
    "s_add_u32 s54, s54, 327680\n"
    "v_lshl_add_u32 v144, v241, 12, s54\n"
    "v_lshrrev_b32 v241, 4, v1\n"
    "s_add_u32 m0, s47, 32768\n"
    "s_nop 0\n"
    "global_load_lds_dwordx4 v9, s[62:63]\n"
    "v_lshl_add_u32 v144, v241, 5, v144\n"
    "v_lshl_add_u32 v147, s30, 11, v2\n"
    "v_add_u32 v147, 147456, v147\n"
    "s_add_u32 m0, s44, 49152\n"
    "s_add_u32 s62, s10, s43\n"
    "s_addc_u32 s63, s11, 0\n"
    "global_load_lds_dwordx4 v6, s[62:63]\n"
    "s_add_u32 m0, s45, 49152\n"
    "s_nop 0\n"
    "global_load_lds_dwordx4 v7, s[62:63]\n"
    "s_add_u32 m0, s46, 49152\n"
    "s_nop 0\n"
    "global_load_lds_dwordx4 v8, s[62:63]\n"
    "s_add_u32 m0, s47, 49152\n"
    "s_nop 0\n"
    "global_load_lds_dwordx4 v9, s[62:63]\n"
    "s_add_u32 s40, s40, 1024\n"
    "s_add_u32 s41, s41, 1024\n"
    "s_add_u32 s42, s42, 1024\n"
    "s_add_u32 s43, s43, 1024\n"
    "s_waitcnt vmcnt(14)\n"
    "v_dot2c_f32_bf16 v120, s49, v56\n"
    "v_dot2c_f32_bf16 v121, v56, v56\n"
    "v_lshlrev_b32 v16, 16, v56\n"
    "v_and_b32 v17, s50, v56\n"
    "v_dot2c_f32_bf16 v120, s49, v57\n"
    "v_dot2c_f32_bf16 v121, v57, v57\n"
    "v_lshlrev_b32 v18, 16, v57\n"
    "v_and_b32 v19, s50, v57\n"
    "v_dot2c_f32_bf16 v120, s49, v58\n"
    "v_dot2c_f32_bf16 v121, v58, v58\n"
    "v_lshlrev_b32 v20, 16, v58\n"
    "v_and_b32 v21, s50, v58\n"
    "v_dot2c_f32_bf16 v120, s49, v59\n"
    "v_dot2c_f32_bf16 v121, v59, v59\n"
    "v_lshlrev_b32 v22, 16, v59\n"
    "v_and_b32 v23, s50, v59\n"
    "v_dot2c_f32_bf16 v122, s49, v60\n"
    "v_dot2c_f32_bf16 v123, v60, v60\n"
    "v_lshlrev_b32 v24, 16, v60\n"
    "v_and_b32 v25, s50, v60\n"
    "v_dot2c_f32_bf16 v122, s49, v61\n"
    "v_dot2c_f32_bf16 v123, v61, v61\n"
    "v_lshlrev_b32 v26, 16, v61\n"
    "v_and_b32 v27, s50, v61\n"
    "v_dot2c_f32_bf16 v122, s49, v62\n"
    "v_dot2c_f32_bf16 v123, v62, v62\n"
    "v_lshlrev_b32 v28, 16, v62\n"
    "v_and_b32 v29, s50, v62\n"
    "v_dot2c_f32_bf16 v122, s49, v63\n"
    "v_dot2c_f32_bf16 v123, v63, v63\n"
    "v_lshlrev_b32 v30, 16, v63\n"
    "v_and_b32 v31, s50, v63\n"
    "v_dot2c_f32_bf16 v124, s49, v64\n"
    "v_dot2c_f32_bf16 v125, v64, v64\n"
    "v_lshlrev_b32 v32, 16, v64\n"
    "v_and_b32 v33, s50, v64\n"
    "v_dot2c_f32_bf16 v124, s49, v65\n"
    "v_dot2c_f32_bf16 v125, v65, v65\n"
    "v_lshlrev_b32 v34, 16, v65\n"
    "v_and_b32 v35, s50, v65\n"
    "v_dot2c_f32_bf16 v124, s49, v66\n"
    "v_dot2c_f32_bf16 v125, v66, v66\n"
    "v_lshlrev_b32 v36, 16, v66\n"
    "v_and_b32 v37, s50, v66\n"
    "v_dot2c_f32_bf16 v124, s49, v67\n"
    "v_dot2c_f32_bf16 v125, v67, v67\n"
    "v_lshlrev_b32 v38, 16, v67\n"
    "v_and_b32 v39, s50, v67\n"
    "v_dot2c_f32_bf16 v126, s49, v68\n"
    "v_dot2c_f32_bf16 v127, v68, v68\n"
    "v_lshlrev_b32 v40, 16, v68\n"
    "v_and_b32 v41, s50, v68\n"
    "v_dot2c_f32_bf16 v126, s49, v69\n"
    "v_dot2c_f32_bf16 v127, v69, v69\n"
    "v_lshlrev_b32 v42, 16, v69\n"
    "v_and_b32 v43, s50, v69\n"
    "v_dot2c_f32_bf16 v126, s49, v70\n"
    "v_dot2c_f32_bf16 v127, v70, v70\n"
    "v_lshlrev_b32 v44, 16, v70\n"
    "v_and_b32 v45, s50, v70\n"
    "v_dot2c_f32_bf16 v126, s49, v71\n"
    "v_dot2c_f32_bf16 v127, v71, v71\n"
    "v_lshlrev_b32 v46, 16, v71\n"
    "v_and_b32 v47, s50, v71\n"
    "v_dot2c_f32_bf16 v128, s49, v72\n"
    "v_dot2c_f32_bf16 v129, v72, v72\n"
    "v_lshlrev_b32 v48, 16, v72\n"
    "v_and_b32 v49, s50, v72\n"
    "v_dot2c_f32_bf16 v128, s49, v73\n"
    "v_dot2c_f32_bf16 v129, v73, v73\n"
    "v_lshlrev_b32 v50, 16, v73\n"
    "v_and_b32 v51, s50, v73\n"
    "v_dot2c_f32_bf16 v128, s49, v74\n"
    "v_dot2c_f32_bf16 v129, v74, v74\n"
    "v_lshlrev_b32 v52, 16, v74\n"
    "v_and_b32 v53, s50, v74\n"
    "v_dot2c_f32_bf16 v128, s49, v75\n"
    "v_dot2c_f32_bf16 v129, v75, v75\n"
    "v_lshlrev_b32 v54, 16, v75\n"
    "v_and_b32 v55, s50, v75\n"
    "v_dot2c_f32_bf16 v130, s49, v76\n"
    "v_dot2c_f32_bf16 v131, v76, v76\n"
    "v_lshlrev_b32 v184, 16, v76\n"
    "v_and_b32 v185, s50, v76\n"
    "v_dot2c_f32_bf16 v130, s49, v77\n"
    "v_dot2c_f32_bf16 v131, v77, v77\n"
    "v_lshlrev_b32 v186, 16, v77\n"
    "v_and_b32 v187, s50, v77\n"
    "v_dot2c_f32_bf16 v130, s49, v78\n"
    "v_dot2c_f32_bf16 v131, v78, v78\n"
    "v_lshlrev_b32 v188, 16, v78\n"
    "v_and_b32 v189, s50, v78\n"
    "v_dot2c_f32_bf16 v130, s49, v79\n"
    "v_dot2c_f32_bf16 v131, v79, v79\n"
    "v_lshlrev_b32 v190, 16, v79\n"
    "v_and_b32 v191, s50, v79\n"
    "v_dot2c_f32_bf16 v132, s49, v80\n"
    "v_dot2c_f32_bf16 v133, v80, v80\n"
    "v_lshlrev_b32 v192, 16, v80\n"
    "v_and_b32 v193, s50, v80\n"
    "v_dot2c_f32_bf16 v132, s49, v81\n"
    "v_dot2c_f32_bf16 v133, v81, v81\n"
    "v_lshlrev_b32 v194, 16, v81\n"
    "v_and_b32 v195, s50, v81\n"
    "v_dot2c_f32_bf16 v132, s49, v82\n"
    "v_dot2c_f32_bf16 v133, v82, v82\n"
    "v_lshlrev_b32 v196, 16, v82\n"
    "v_and_b32 v197, s50, v82\n"
    "v_dot2c_f32_bf16 v132, s49, v83\n"
    "v_dot2c_f32_bf16 v133, v83, v83\n"
    "v_lshlrev_b32 v198, 16, v83\n"
    "v_and_b32 v199, s50, v83\n"
    "v_dot2c_f32_bf16 v134, s49, v84\n"
    "v_dot2c_f32_bf16 v135, v84, v84\n"
    "v_lshlrev_b32 v200, 16, v84\n"
    "v_and_b32 v201, s50, v84\n"
    "v_dot2c_f32_bf16 v134, s49, v85\n"
    "v_dot2c_f32_bf16 v135, v85, v85\n"
    "v_lshlrev_b32 v202, 16, v85\n"
    "v_and_b32 v203, s50, v85\n"
    "v_dot2c_f32_bf16 v134, s49, v86\n"
    "v_dot2c_f32_bf16 v135, v86, v86\n"
    "v_lshlrev_b32 v204, 16, v86\n"
    "v_and_b32 v205, s50, v86\n"
    "v_dot2c_f32_bf16 v134, s49, v87\n"
    "v_dot2c_f32_bf16 v135, v87, v87\n"
    "v_lshlrev_b32 v206, 16, v87\n"
    "v_and_b32 v207, s50, v87\n"
    "global_load_dwordx4 v[208:211], v230, s[16:17] sc1\n"
    "v_add_u32 v231, 24576, v230\n"
    "global_load_dwordx4 v[212:215], v231, s[16:17] sc1\n"
    "v_add_u32 v230, 24576, v231\n"
    "global_load_dwordx4 v[216:219], v230, s[16:17] sc1\n"
    "v_add_u32 v231, 24576, v230\n"
    "global_load_dwordx4 v[220:223], v231, s[16:17] sc1\n"
    "v_add_u32 v230, -73728, v231\n"
    "s_waitcnt vmcnt(4)\n"
    "s_barrier\n"
    "global_load_dword v4, v139, s[60:61]\n"
    "v_permlane32_swap_b32 v120, v128\n"
    "v_permlane32_swap_b32 v121, v129\n"
    "v_permlane32_swap_b32 v122, v130\n"
    "v_permlane32_swap_b32 v123, v131\n"
    "v_permlane32_swap_b32 v124, v132\n"
    "v_permlane32_swap_b32 v125, v133\n"
    "v_permlane32_swap_b32 v126, v134\n"
    "v_permlane32_swap_b32 v127, v135\n"
    "v_pk_add_f32 v[120:121], v[120:121], v[128:129]\n"
    "v_pk_add_f32 v[122:123], v[122:123], v[130:131]\n"
    "v_pk_add_f32 v[124:125], v[124:125], v[132:133]\n"
    "v_pk_add_f32 v[126:127], v[126:127], v[134:135]\n"
    "s_nop 0\n"
    "v_permlane16_swap_b32 v120, v124\n"
    "v_permlane16_swap_b32 v121, v125\n"
    "v_permlane16_swap_b32 v122, v126\n"
    "v_permlane16_swap_b32 v123, v127\n"
    "v_pk_add_f32 v[120:121], v[120:121], v[124:125]\n"
    "v_pk_add_f32 v[122:123], v[122:123], v[126:127]\n"
    "s_nop 0\n"
    "v_add_f32_dpp v120, v120, v120 row_ror:8 row_mask:0xf bank_mask:0xf bound_ctrl:1\n"
    "v_add_f32_dpp v121, v121, v121 row_ror:8 row_mask:0xf bank_mask:0xf bound_ctrl:1\n"
    "v_add_f32_dpp v122, v122, v122 row_ror:8 row_mask:0xf bank_mask:0xf bound_ctrl:1\n"
    "v_add_f32_dpp v123, v123, v123 row_ror:8 row_mask:0xf bank_mask:0xf bound_ctrl:1\n"
    "v_add_f32_dpp v120, v120, v120 row_ror:4 row_mask:0xf bank_mask:0xf bound_ctrl:1\n"
    "v_add_f32_dpp v121, v121, v121 row_ror:4 row_mask:0xf bank_mask:0xf bound_ctrl:1\n"
    "v_add_f32_dpp v122, v122, v122 row_ror:4 row_mask:0xf bank_mask:0xf bound_ctrl:1\n"
    "v_add_f32_dpp v123, v123, v123 row_ror:4 row_mask:0xf bank_mask:0xf bound_ctrl:1\n"
    "v_add_f32_dpp v120, v120, v120 row_ror:2 row_mask:0xf bank_mask:0xf bound_ctrl:1\n"
    "v_add_f32_dpp v121, v121, v121 row_ror:2 row_mask:0xf bank_mask:0xf bound_ctrl:1\n"
    "v_add_f32_dpp v122, v122, v122 row_ror:2 row_mask:0xf bank_mask:0xf bound_ctrl:1\n"
    "v_add_f32_dpp v123, v123, v123 row_ror:2 row_mask:0xf bank_mask:0xf bound_ctrl:1\n"
    "v_add_f32_dpp v120, v120, v120 row_ror:1 row_mask:0xf bank_mask:0xf bound_ctrl:1\n"
    "v_add_f32_dpp v121, v121, v121 row_ror:1 row_mask:0xf bank_mask:0xf bound_ctrl:1\n"
    "v_add_f32_dpp v122, v122, v122 row_ror:1 row_mask:0xf bank_mask:0xf bound_ctrl:1\n"
    "v_add_f32_dpp v123, v123, v123 row_ror:1 row_mask:0xf bank_mask:0xf bound_ctrl:1\n"
    "v_pk_mul_f32 v[120:121], v[120:121], s[98:99] op_sel_hi:[1,0]\n"
    "v_pk_mul_f32 v[122:123], v[122:123], s[98:99] op_sel_hi:[1,0]\n"
    "v_fma_f32 v121, -v120, v120, v121\n"
    "v_fma_f32 v123, -v122, v122, v123\n"
    "v_max_f32 v121, 0, v121\n"
    "v_max_f32 v123, 0, v123\n"
    "v_add_f32 v121, 0x3727c5ac, v121\n"
    "v_add_f32 v123, 0x3727c5ac, v123\n"
    "v_rsq_f32 v121, v121\n"
    "v_rsq_f32 v123, v123\n"
    "v_mul_f32_e64 v120, -v120, v121\n"
    "v_mul_f32_e64 v122, -v122, v123\n"
    "v_readlane_b32 s69, v121, 0\n"
    "v_readlane_b32 s68, v120, 0\n"
    "v_readlane_b32 s71, v123, 0\n"
    "v_readlane_b32 s70, v122, 0\n"
    "v_readlane_b32 s73, v121, 16\n"
    "v_readlane_b32 s72, v120, 16\n"
    "v_readlane_b32 s75, v123, 16\n"
    "v_readlane_b32 s74, v122, 16\n"
    "v_readlane_b32 s77, v121, 32\n"
    "v_readlane_b32 s76, v120, 32\n"
    "v_readlane_b32 s79, v123, 32\n"
    "v_readlane_b32 s78, v122, 32\n"
    "v_readlane_b32 s81, v121, 48\n"
    "v_readlane_b32 s80, v120, 48\n"
    "v_readlane_b32 s83, v123, 48\n"
    "v_readlane_b32 s82, v122, 48\n"
    "s_cmp_eq_u32 s99, 0\n"
    "s_cbranch_scc1 .LHn0\n"
    "global_load_dwordx4 v[88:91], v160, s[12:13] offset:-3072\n"
    "global_load_dwordx4 v[92:95], v160, s[12:13] offset:3072\n"
    "global_load_dwordx4 v[96:99], v161, s[12:13] offset:-3072\n"
    "global_load_dwordx4 v[100:103], v161, s[12:13] offset:3072\n"
    "global_load_ushort v104, v162, s[14:15]\n"
    ".LHn0:\n"
    "v_pk_fma_f32 v[16:17], v[16:17], s[68:69], s[68:69] op_sel:[0,1,0] op_sel_hi:[1,1,0]\n"
    "v_pk_fma_f32 v[18:19], v[18:19], s[68:69], s[68:69] op_sel:[0,1,0] op_sel_hi:[1,1,0]\n"
    "v_pk_fma_f32 v[20:21], v[20:21], s[68:69], s[68:69] op_sel:[0,1,0] op_sel_hi:[1,1,0]\n"
    "v_pk_fma_f32 v[22:23], v[22:23], s[68:69], s[68:69] op_sel:[0,1,0] op_sel_hi:[1,1,0]\n"
    "v_pk_fma_f32 v[24:25], v[24:25], s[70:71], s[70:71] op_sel:[0,1,0] op_sel_hi:[1,1,0]\n"
    "v_pk_fma_f32 v[26:27], v[26:27], s[70:71], s[70:71] op_sel:[0,1,0] op_sel_hi:[1,1,0]\n"
    "v_pk_fma_f32 v[28:29], v[28:29], s[70:71], s[70:71] op_sel:[0,1,0] op_sel_hi:[1,1,0]\n"
    "v_pk_fma_f32 v[30:31], v[30:31], s[70:71], s[70:71] op_sel:[0,1,0] op_sel_hi:[1,1,0]\n"
    "v_pk_fma_f32 v[32:33], v[32:33], s[72:73], s[72:73] op_sel:[0,1,0] op_sel_hi:[1,1,0]\n"
    "v_pk_fma_f32 v[34:35], v[34:35], s[72:73], s[72:73] op_sel:[0,1,0] op_sel_hi:[1,1,0]\n"
    "v_pk_fma_f32 v[36:37], v[36:37], s[72:73], s[72:73] op_sel:[0,1,0] op_sel_hi:[1,1,0]\n"
    "v_pk_fma_f32 v[38:39], v[38:39], s[72:73], s[72:73] op_sel:[0,1,0] op_sel_hi:[1,1,0]\n"
    "v_pk_fma_f32 v[40:41], v[40:41], s[74:75], s[74:75] op_sel:[0,1,0] op_sel_hi:[1,1,0]\n"
    "v_pk_fma_f32 v[42:43], v[42:43], s[74:75], s[74:75] op_sel:[0,1,0] op_sel_hi:[1,1,0]\n"
    "v_pk_fma_f32 v[44:45], v[44:45], s[74:75], s[74:75] op_sel:[0,1,0] op_sel_hi:[1,1,0]\n"
    "v_pk_fma_f32 v[46:47], v[46:47], s[74:75], s[74:75] op_sel:[0,1,0] op_sel_hi:[1,1,0]\n"
    "v_pk_fma_f32 v[48:49], v[48:49], s[76:77], s[76:77] op_sel:[0,1,0] op_sel_hi:[1,1,0]\n"
    "v_pk_fma_f32 v[50:51], v[50:51], s[76:77], s[76:77] op_sel:[0,1,0] op_sel_hi:[1,1,0]\n"
    "v_pk_fma_f32 v[52:53], v[52:53], s[76:77], s[76:77] op_sel:[0,1,0] op_sel_hi:[1,1,0]\n"
    "v_pk_fma_f32 v[54:55], v[54:55], s[76:77], s[76:77] op_sel:[0,1,0] op_sel_hi:[1,1,0]\n"
    "v_pk_fma_f32 v[184:185], v[184:185], s[78:79], s[78:79] op_sel:[0,1,0] op_sel_hi:[1,1,0]\n"
    "v_pk_fma_f32 v[186:187], v[186:187], s[78:79], s[78:79] op_sel:[0,1,0] op_sel_hi:[1,1,0]\n"
    "v_pk_fma_f32 v[188:189], v[188:189], s[78:79], s[78:79] op_sel:[0,1,0] op_sel_hi:[1,1,0]\n"
    "v_pk_fma_f32 v[190:191], v[190:191], s[78:79], s[78:79] op_sel:[0,1,0] op_sel_hi:[1,1,0]\n"
    "v_pk_fma_f32 v[192:193], v[192:193], s[80:81], s[80:81] op_sel:[0,1,0] op_sel_hi:[1,1,0]\n"
    "v_pk_fma_f32 v[194:195], v[194:195], s[80:81], s[80:81] op_sel:[0,1,0] op_sel_hi:[1,1,0]\n"
    "v_pk_fma_f32 v[196:197], v[196:197], s[80:81], s[80:81] op_sel:[0,1,0] op_sel_hi:[1,1,0]\n"
    "v_pk_fma_f32 v[198:199], v[198:199], s[80:81], s[80:81] op_sel:[0,1,0] op_sel_hi:[1,1,0]\n"
    "v_pk_fma_f32 v[200:201], v[200:201], s[82:83], s[82:83] op_sel:[0,1,0] op_sel_hi:[1,1,0]\n"
    "v_pk_fma_f32 v[202:203], v[202:203], s[82:83], s[82:83] op_sel:[0,1,0] op_sel_hi:[1,1,0]\n"
    "v_pk_fma_f32 v[204:205], v[204:205], s[82:83], s[82:83] op_sel:[0,1,0] op_sel_hi:[1,1,0]\n"
    "v_pk_fma_f32 v[206:207], v[206:207], s[82:83], s[82:83] op_sel:[0,1,0] op_sel_hi:[1,1,0]\n"
    "s_cmp_eq_u32 s99, 0\n"
    "s_cbranch_scc1 .LHn4\n"
    "s_waitcnt vmcnt(6)\n"
    "s_branch .LHn3\n"
    ".LHn4:\n"
    "s_waitcnt vmcnt(1)\n"
    ".LHn3:\n"
    "v_xor_b32 v239, s38, v209\n"
    "v_bitop3_b32 v239, v239, v211, s38 bitop3:0xf6\n"
    "v_bitop3_b32 v239, v239, v213, s38 bitop3:0xf6\n"
    "v_bitop3_b32 v239, v239, v215, s38 bitop3:0xf6\n"
    "v_bitop3_b32 v239, v239, v217, s38 bitop3:0xf6\n"
    "v_bitop3_b32 v239, v239, v219, s38 bitop3:0xf6\n"
    "v_bitop3_b32 v239, v239, v221, s38 bitop3:0xf6\n"
    "v_bitop3_b32 v239, v239, v223, s38 bitop3:0xf6\n"
    "v_cmp_ne_u32 vcc, 0, v239\n"
    "s_cbranch_vccnz .Lmr1\n"
    ".Lmb2:\n"
    "v_add_f32 v208, v208, v212\n"
    "v_add_f32 v210, v210, v214\n"
    "v_add_f32 v216, v216, v220\n"
    "v_add_f32 v218, v218, v222\n"
    "v_add_f32 v208, v208, v216\n"
    "v_add_f32 v210, v210, v218\n"
    "v_cvt_pk_bf16_f32 v209, v208, v210\n"
    "v_lshlrev_b32 v208, 16, v209\n"
    "v_and_b32 v209, s50, v209\n"
    "ds_write_b64 v232, v[208:209] offset:2048\n"
    "s_waitcnt lgkmcnt(0)\n"
    "s_barrier\n"
    "ds_read_b128 v[208:211], v228 offset:0\n"
    "ds_read_b128 v[216:219], v228 offset:2048\n"
    "ds_read_b128 v[212:215], v228 offset:1024\n"
    "ds_read_b128 v[220:223], v228 offset:3072\n"
    "s_waitcnt lgkmcnt(2)\n"
    "v_pk_fma_f32 v[16:17], v[16:17], v[208:209], v[216:217]\n"
    "v_pk_fma_f32 v[18:19], v[18:19], v[210:211], v[218:219]\n"
    "s_waitcnt lgkmcnt(0)\n"
    "v_pk_fma_f32 v[20:21], v[20:21], v[212:213], v[220:221]\n"
    "v_pk_fma_f32 v[22:23], v[22:23], v[214:215], v[222:223]\n"
    "v_cvt_pk_bf16_f32 v16, v16, v17\n"
    "v_cvt_pk_bf16_f32 v17, v18, v19\n"
    "v_cvt_pk_bf16_f32 v18, v20, v21\n"
    "v_cvt_pk_bf16_f32 v19, v22, v23\n"
    "ds_write_b128 v224, v[16:19] offset:0\n"
    "v_pk_fma_f32 v[24:25], v[24:25], v[208:209], v[216:217]\n"
    "v_pk_fma_f32 v[26:27], v[26:27], v[210:211], v[218:219]\n"
    "v_pk_fma_f32 v[28:29], v[28:29], v[212:213], v[220:221]\n"
    "v_pk_fma_f32 v[30:31], v[30:31], v[214:215], v[222:223]\n"
    "v_cvt_pk_bf16_f32 v24, v24, v25\n"
    "v_cvt_pk_bf16_f32 v25, v26, v27\n"
    "v_cvt_pk_bf16_f32 v26, v28, v29\n"
    "v_cvt_pk_bf16_f32 v27, v30, v31\n"
    "ds_write_b128 v224, v[24:27] offset:16\n"
    "v_pk_fma_f32 v[32:33], v[32:33], v[208:209], v[216:217]\n"
    "v_pk_fma_f32 v[34:35], v[34:35], v[210:211], v[218:219]\n"
    "v_pk_fma_f32 v[36:37], v[36:37], v[212:213], v[220:221]\n"
    "v_pk_fma_f32 v[38:39], v[38:39], v[214:215], v[222:223]\n"
    "v_cvt_pk_bf16_f32 v32, v32, v33\n"
    "v_cvt_pk_bf16_f32 v33, v34, v35\n"
    "v_cvt_pk_bf16_f32 v34, v36, v37\n"
    "v_cvt_pk_bf16_f32 v35, v38, v39\n"
    "ds_write_b128 v225, v[32:35] offset:0\n"
    "v_pk_fma_f32 v[40:41], v[40:41], v[208:209], v[216:217]\n"
    "v_pk_fma_f32 v[42:43], v[42:43], v[210:211], v[218:219]\n"
    "v_pk_fma_f32 v[44:45], v[44:45], v[212:213], v[220:221]\n"
    "v_pk_fma_f32 v[46:47], v[46:47], v[214:215], v[222:223]\n"
    "v_cvt_pk_bf16_f32 v40, v40, v41\n"
    "v_cvt_pk_bf16_f32 v41, v42, v43\n"
    "v_cvt_pk_bf16_f32 v42, v44, v45\n"
    "v_cvt_pk_bf16_f32 v43, v46, v47\n"
    "ds_write_b128 v225, v[40:43] offset:16\n"
    "v_pk_fma_f32 v[48:49], v[48:49], v[208:209], v[216:217]\n"
    "v_pk_fma_f32 v[50:51], v[50:51], v[210:211], v[218:219]\n"
    "v_pk_fma_f32 v[52:53], v[52:53], v[212:213], v[220:221]\n"
    "v_pk_fma_f32 v[54:55], v[54:55], v[214:215], v[222:223]\n"
    "v_cvt_pk_bf16_f32 v48, v48, v49\n"
    "v_cvt_pk_bf16_f32 v49, v50, v51\n"
    "v_cvt_pk_bf16_f32 v50, v52, v53\n"
    "v_cvt_pk_bf16_f32 v51, v54, v55\n"
    "ds_write_b128 v226, v[48:51] offset:0\n"
    "v_pk_fma_f32 v[184:185], v[184:185], v[208:209], v[216:217]\n"
    "v_pk_fma_f32 v[186:187], v[186:187], v[210:211], v[218:219]\n"
    "v_pk_fma_f32 v[188:189], v[188:189], v[212:213], v[220:221]\n"
    "v_pk_fma_f32 v[190:191], v[190:191], v[214:215], v[222:223]\n"
    "v_cvt_pk_bf16_f32 v184, v184, v185\n"
    "v_cvt_pk_bf16_f32 v185, v186, v187\n"
    "v_cvt_pk_bf16_f32 v186, v188, v189\n"
    "v_cvt_pk_bf16_f32 v187, v190, v191\n"
    "ds_write_b128 v226, v[184:187] offset:16\n"
    "v_pk_fma_f32 v[192:193], v[192:193], v[208:209], v[216:217]\n"
    "v_pk_fma_f32 v[194:195], v[194:195], v[210:211], v[218:219]\n"
    "v_pk_fma_f32 v[196:197], v[196:197], v[212:213], v[220:221]\n"
    "v_pk_fma_f32 v[198:199], v[198:199], v[214:215], v[222:223]\n"
    "v_cvt_pk_bf16_f32 v192, v192, v193\n"
    "v_cvt_pk_bf16_f32 v193, v194, v195\n"
    "v_cvt_pk_bf16_f32 v194, v196, v197\n"
    "v_cvt_pk_bf16_f32 v195, v198, v199\n"
    "ds_write_b128 v227, v[192:195] offset:0\n"
    "v_pk_fma_f32 v[200:201], v[200:201], v[208:209], v[216:217]\n"
    "v_pk_fma_f32 v[202:203], v[202:203], v[210:211], v[218:219]\n"
    "v_pk_fma_f32 v[204:205], v[204:205], v[212:213], v[220:221]\n"
    "v_pk_fma_f32 v[206:207], v[206:207], v[214:215], v[222:223]\n"
    "v_cvt_pk_bf16_f32 v200, v200, v201\n"
    "v_cvt_pk_bf16_f32 v201, v202, v203\n"
    "v_cvt_pk_bf16_f32 v202, v204, v205\n"
    "v_cvt_pk_bf16_f32 v203, v206, v207\n"
    "ds_write_b128 v227, v[200:203] offset:16\n"
    "s_waitcnt lgkmcnt(0)\n"
    "s_barrier\n"
    "s_add_u32 m0, s44, 0\n"
    "s_add_u32 s62, s10, s40\n"
    "s_addc_u32 s63, s11, 0\n"
    "global_load_lds_dwordx4 v6, s[62:63]\n"
    "s_add_u32 m0, s45, 0\n"
    "s_nop 0\n"
    "global_load_lds_dwordx4 v7, s[62:63]\n"
    "s_add_u32 m0, s46, 0\n"
    "s_nop 0\n"
    "global_load_lds_dwordx4 v8, s[62:63]\n"
    "s_add_u32 m0, s47, 0\n"
    "s_nop 0\n"
    "global_load_lds_dwordx4 v9, s[62:63]\n"
    "s_add_u32 m0, s44, 16384\n"
    "s_add_u32 s62, s10, s41\n"
    "s_addc_u32 s63, s11, 0\n"
    "global_load_lds_dwordx4 v6, s[62:63]\n"
    "s_add_u32 m0, s45, 16384\n"
    "s_nop 0\n"
    "global_load_lds_dwordx4 v7, s[62:63]\n"
    "s_add_u32 m0, s46, 16384\n"
    "s_nop 0\n"
    "global_load_lds_dwordx4 v8, s[62:63]\n"
    "s_mov_b32 s64, 0xffffffff\n"
    "s_mov_b32 s65, 0xfffffff0\n"
    "s_mov_b32 s66, 0xfffffffe\n"
    "s_waitcnt vmcnt(6)\n"
    "s_add_u32 m0, s47, 16384\n"
    "s_nop 0\n"
    "global_load_lds_dwordx4 v9, s[62:63]\n"
    "s_mov_b32 s67, 0xffffffe0\n"
    "s_mov_b32 s68, 0xfffffffc\n"
    "s_mov_b32 s69, 0xffffffc0\n"
    "v_lshlrev_b32 v104, 16, v104\n"
    "s_mov_b32 s58, 0xff00ff00\n"
    "s_mov_b32 s59, 0xff00ff00\n"
    "v_mul_f32 v238, 0xbfb8aa3b, v104\n"
    "v_exp_f32 v238, v238\n"
    "s_nop 0\n"
    "v_add_f32 v238, 1.0, v238\n"
    "v_rcp_f32 v238, v238\n"
    "s_nop 0\n"
    "v_mul_f32 v238, v238, v104\n"
    "v_cvt_pk_bf16_f32 v104, v238, 0\n"
    "s_waitcnt vmcnt(6)\n"
    "s_add_u32 m0, s44, 32768\n"
    "s_add_u32 s62, s10, s42\n"
    "s_addc_u32 s63, s11, 0\n"
    "global_load_lds_dwordx4 v6, s[62:63]\n"
    "s_mov_b32 s70, 0xfffffff8\n"
    "s_mov_b32 s71, 0xffffff80\n"
    "s_mov_b32 s72, 0xffffff00\n"
    "v_mov_b32_dpp v238, v104 quad_perm:[0,0,0,0] row_mask:0xf bank_mask:0xf bound_ctrl:1\n"
    "v_mov_b32_dpp v239, v104 quad_perm:[1,1,1,1] row_mask:0xf bank_mask:0xf bound_ctrl:1\n"
    "v_mov_b32_dpp v240, v104 quad_perm:[2,2,2,2] row_mask:0xf bank_mask:0xf bound_ctrl:1\n"
    "v_mov_b32_dpp v241, v104 quad_perm:[3,3,3,3] row_mask:0xf bank_mask:0xf bound_ctrl:1\n"
    "v_lshlrev_b32 v242, 16, v238\n"
    "v_lshlrev_b32 v243, 16, v239\n"
    "v_lshlrev_b32 v244, 16, v240\n"
    "v_lshlrev_b32 v245, 16, v241\n"
    "v_lshlrev_b32 v238, 16, v88\n"
    "s_waitcnt vmcnt(6)\n"
    "s_add_u32 m0, s45, 32768\n"
    "s_nop 0\n"
    "global_load_lds_dwordx4 v7, s[62:63]\n"
    "s_mov_b32 s73, 0xfffff000\n"
    "s_mov_b32 s74, 0xfffffe00\n"
    "s_mov_b32 s75, 0xffffe000\n"
    "v_and_b32 v88, s50, v88\n"
    "v_fmac_f32 v106, v238, v242\n"
    "v_fmac_f32 v107, v88, v242\n"
    "v_lshlrev_b32 v238, 16, v89\n"
    "v_and_b32 v89, s50, v89\n"
    "v_fmac_f32 v108, v238, v242\n"
    "v_fmac_f32 v109, v89, v242\n"
    "v_lshlrev_b32 v238, 16, v90\n"
    "v_and_b32 v90, s50, v90\n"
    "s_waitcnt vmcnt(6)\n"
    "s_add_u32 m0, s46, 32768\n"
    "s_nop 0\n"
    "global_load_lds_dwordx4 v8, s[62:63]\n"
    "s_mov_b32 s76, 0xfffffc00\n"
    "s_mov_b32 s77, 0xffffc000\n"
    "s_mov_b32 s78, 0xfffff800\n"
    "v_fmac_f32 v110, v238, v242\n"
    "v_fmac_f32 v111, v90, v242\n"
    "v_lshlrev_b32 v238, 16, v91\n"
    "v_and_b32 v91, s50, v91\n"
    "v_fmac_f32 v112, v238, v242\n"
    "v_fmac_f32 v113, v91, v242\n"
    "v_lshlrev_b32 v238, 16, v92\n"
    "v_and_b32 v92, s50, v92\n"
    "v_fmac_f32 v106, v238, v243\n"
    "s_waitcnt vmcnt(6)\n"
    "s_add_u32 m0, s47, 32768\n"
    "s_nop 0\n"
    "global_load_lds_dwordx4 v9, s[62:63]\n"
    "s_mov_b32 s79, 0xffff8000\n"
    "s_mov_b32 s80, 0xffff0000\n"
    "s_mov_b32 s81, 0xfff00000\n"
    "v_fmac_f32 v107, v92, v243\n"
    "v_lshlrev_b32 v238, 16, v93\n"
    "v_and_b32 v93, s50, v93\n"
    "v_fmac_f32 v108, v238, v243\n"
    "v_fmac_f32 v109, v93, v243\n"
    "v_lshlrev_b32 v238, 16, v94\n"
    "v_and_b32 v94, s50, v94\n"
    "v_fmac_f32 v110, v238, v243\n"
    "v_fmac_f32 v111, v94, v243\n"
    "s_waitcnt vmcnt(6)\n"
    "s_add_u32 m0, s44, 49152\n"
    "s_add_u32 s62, s10, s43\n"
    "s_addc_u32 s63, s11, 0\n"
    "global_load_lds_dwordx4 v6, s[62:63]\n"
    "s_mov_b32 s82, 0xfffe0000\n"
    "s_mov_b32 s83, 0xffe00000\n"
    "s_mov_b32 s84, 0xfffc0000\n"
    "v_lshlrev_b32 v238, 16, v95\n"
    "v_and_b32 v95, s50, v95\n"
    "v_fmac_f32 v112, v238, v243\n"
    "v_fmac_f32 v113, v95, v243\n"
    "v_lshlrev_b32 v238, 16, v96\n"
    "v_and_b32 v96, s50, v96\n"
    "v_fmac_f32 v106, v238, v244\n"
    "v_fmac_f32 v107, v96, v244\n"
    "v_lshlrev_b32 v238, 16, v97\n"
    "s_waitcnt vmcnt(6)\n"
    "s_add_u32 m0, s45, 49152\n"
    "s_nop 0\n"
    "global_load_lds_dwordx4 v7, s[62:63]\n"
    "s_mov_b32 s85, 0xffc00000\n"
    "s_mov_b32 s86, 0xfff80000\n"
    "s_mov_b32 s87, 0xff800000\n"
    "v_and_b32 v97, s50, v97\n"
    "v_fmac_f32 v108, v238, v244\n"
    "v_fmac_f32 v109, v97, v244\n"
    "v_lshlrev_b32 v238, 16, v98\n"
    "v_and_b32 v98, s50, v98\n"
    "v_fmac_f32 v110, v238, v244\n"
    "v_fmac_f32 v111, v98, v244\n"
    "v_lshlrev_b32 v238, 16, v99\n"
    "v_and_b32 v99, s50, v99\n"
    "s_waitcnt vmcnt(6)\n"
    "s_barrier\n"
    "s_add_u32 m0, s46, 49152\n"
    "s_nop 0\n"
    "global_load_lds_dwordx4 v8, s[62:63]\n"
    "s_mov_b32 s88, 0xff000000\n"
    "s_mov_b32 s89, 0xf0000000\n"
    "s_mov_b32 s90, 0xfe000000\n"
    "v_fmac_f32 v112, v238, v244\n"
    "s_waitcnt vmcnt(6)\n"
    "s_add_u32 m0, s47, 49152\n"
    "s_nop 0\n"
    "global_load_lds_dwordx4 v9, s[62:63]\n"
    "s_mov_b32 s91, 0xe0000000\n"
    "s_mov_b32 s92, 0xfc000000\n"
    "s_mov_b32 s93, 0xc0000000\n"
    "v_fmac_f32 v113, v99, v244\n"
    "s_waitcnt vmcnt(6)\n"
    "s_sub_u32 s40, s40, 2048\n"
    "s_sub_u32 s41, s41, 2048\n"
    "s_sub_u32 s42, s42, 2048\n"
    "s_sub_u32 s43, s43, 2048\n"
    "s_add_u32 m0, s44, 65536\n"
    "s_add_u32 s62, s10, s40\n"
    "s_addc_u32 s63, s11, 0\n"
    "global_load_lds_dwordx4 v6, s[62:63]\n"
    "s_mov_b32 s94, 0xf8000000\n"
    "s_mov_b32 s95, 0x80000000\n"
    "s_lshl_b32 s6, s37, 7\n"
    "v_lshlrev_b32 v238, 16, v100\n"
    "s_waitcnt vmcnt(6)\n"
    "s_add_u32 m0, s45, 65536\n"
    "s_nop 0\n"
    "global_load_lds_dwordx4 v7, s[62:63]\n"
    "s_lshr_b32 s7, s32, 3\n"
    "s_lshl4_add_u32 s7, s7, s30\n"
    "s_lshl_b32 s7, s7, 10\n"
    "v_and_b32 v100, s50, v100\n"
    "s_waitcnt vmcnt(6)\n"
    "s_add_u32 m0, s46, 65536\n"
    "s_nop 0\n"
    "global_load_lds_dwordx4 v8, s[62:63]\n"
    "s_lshl3_add_u32 s12, s36, s30\n"
    "s_lshl_b32 s12, s12, 13\n"
    "s_add_u32 s12, s12, s6\n"
    "v_fmac_f32 v106, v238, v245\n"
    "s_waitcnt vmcnt(6)\n"
    "s_add_u32 m0, s47, 65536\n"
    "s_nop 0\n"
    "global_load_lds_dwordx4 v9, s[62:63]\n"
    "s_add_u32 s18, s18, s6\n"
    "s_addc_u32 s19, s19, 0\n"
    "s_add_u32 s20, s20, s6\n"
    "s_addc_u32 s21, s21, 0\n"
    "s_add_u32 s8, s8, s12\n"
    "s_addc_u32 s9, s9, 0\n"
    "v_fmac_f32 v107, v100, v245\n"
    "s_waitcnt vmcnt(6)\n"
    "s_add_u32 m0, s44, 82432\n"
    "s_add_u32 s62, s10, s41\n"
    "s_addc_u32 s63, s11, 0\n"
    "global_load_lds_dwordx4 v6, s[62:63]\n"
    "s_lshr_b32 s54, s30, 1\n"
    "s_lshl_b32 s54, s54, 12\n"
    "s_lshl2_add_u32 s54, s6, s54\n"
    "v_lshlrev_b32 v238, 16, v101\n"
    "s_waitcnt vmcnt(6)\n"
    "s_add_u32 m0, s45, 82432\n"
    "s_nop 0\n"
    "global_load_lds_dwordx4 v7, s[62:63]\n"
    "s_and_b32 s55, s30, 1\n"
    "s_lshl_b32 s55, s55, 8\n"
    "s_add_u32 s54, s54, s55\n"
    "v_and_b32 v101, s50, v101\n"
    "s_waitcnt vmcnt(6)\n"
    "s_barrier\n"
    "s_add_u32 m0, s46, 82432\n"
    "s_nop 0\n"
    "global_load_lds_dwordx4 v8, s[62:63]\n"
    "s_add_u32 s15, s54, 12288\n"
    "s_lshr_b32 s54, s30, 2\n"
    "s_lshl1_add_u32 s54, s33, s54\n"
    "v_fmac_f32 v108, v238, v245\n"
    "v_fmac_f32 v109, v101, v245\n"
    "v_lshlrev_b32 v238, 16, v102\n"
    "v_and_b32 v102, s50, v102\n"
    "v_fmac_f32 v110, v238, v245\n"
    "v_fmac_f32 v111, v102, v245\n"
    "v_lshlrev_b32 v238, 16, v103\n"
    "v_and_b32 v103, s50, v103\n"
    "s_waitcnt vmcnt(6)\n"
    "s_add_u32 m0, s47, 82432\n"
    "s_nop 0\n"
    "global_load_lds_dwordx4 v9, s[62:63]\n"
    "s_lshl2_add_u32 s55, s34, s39\n"
    "s_mul_i32 s56, s54, 33024\n"
    "s_lshl_b32 s57, s55, 10\n"
    "v_fmac_f32 v112, v238, v245\n"
    "v_fmac_f32 v113, v103, v245\n"
    "v_permlane32_swap_b32 v106, v110\n"
    "v_permlane32_swap_b32 v107, v111\n"
    "v_permlane32_swap_b32 v108, v112\n"
    "v_permlane32_swap_b32 v109, v113\n"
    "v_add_f32 v106, v106, v110\n"
    "v_add_f32 v107, v107, v111\n"
    "s_waitcnt vmcnt(6)\n"
    "s_add_u32 m0, s44, 99328\n"
    "s_add_u32 s62, s10, s42\n"
    "s_addc_u32 s63, s11, 0\n"
    "global_load_lds_dwordx4 v6, s[62:63]\n"
    "s_add_u32 s56, s56, s57\n"
    "s_add_u32 s13, s56, 7340032\n"
    "s_lshl_b32 s54, s54, 5\n"
    "v_add_f32 v108, v108, v112\n"
    "v_add_f32 v109, v109, v113\n"
    "s_nop 0\n"
    "v_permlane16_swap_b32 v106, v108\n"
    "v_permlane16_swap_b32 v107, v109\n"
    "v_add_f32 v106, v106, v108\n"
    "v_add_f32 v107, v107, v109\n"
    "s_nop 0\n"
    "v_add_f32_dpp v106, v106, v106 row_ror:8 row_mask:0xf bank_mask:0xf bound_ctrl:1\n"
    "v_add_f32_dpp v107, v107, v107 row_ror:8 row_mask:0xf bank_mask:0xf bound_ctrl:1\n"
    "s_waitcnt vmcnt(6)\n"
    "s_add_u32 m0, s45, 99328\n"
    "s_nop 0\n"
    "global_load_lds_dwordx4 v7, s[62:63]\n"
    "s_add_u32 s54, s54, s55\n"
    "s_lshl_b32 s54, s54, 7\n"
    "s_add_u32 s14, s54, 589824\n"
    "v_cndmask_b32_e64 v106, v106, v107, s[58:59]\n"
    "v_mov_b32 v148, s49\n"
    "v_mov_b32 v149, s49\n"
    "v_mov_b32 v150, s49\n"
    "v_mov_b32 v151, s49\n"
    "v_and_b32 v152, 0xf0, v2\n"
    "v_lshlrev_b32 v152, 4, v152\n"
    "v_add_u32 v152, 141312, v152\n"
    "s_waitcnt vmcnt(6)\n"
    "s_add_u32 m0, s46, 99328\n"
    "s_nop 0\n"
    "global_load_lds_dwordx4 v8, s[62:63]\n"
    "s_lshl1_add_u32 s4, s57, 2621440\n"
    "s_mul_i32 s3, s33, 131584\n"
    "s_add_u32 s3, s3, s4\n"
    "v_mov_b32 v243, 0xffff\n"
    "v_mov_b32 v244, s50\n"
    "v_cndmask_b32_e64 v36, 0, v243, s[64:65]\n"
    "v_cndmask_b32_e64 v245, 0, v244, s[66:67]\n"
    "v_or_b32 v36, v36, v245\n"
    "v_cndmask_b32_e64 v37, 0, v243, s[68:69]\n"
    "v_cndmask_b32_e64 v245, 0, v244, s[70:71]\n"
    "v_or_b32 v37, v37, v245\n"
    "s_waitcnt vmcnt(6)\n"
    "s_add_u32 m0, s47, 99328\n"
    "s_nop 0\n"
    "global_load_lds_dwordx4 v9, s[62:63]\n"
    "s_lshl_b32 s1, s30, 13\n"
    "s_and_b32 s56, s30, 1\n"
    "s_lshr_b32 s54, s39, 1\n"
    "v_cndmask_b32_e64 v38, 0, v243, s[72:73]\n"
    "v_cndmask_b32_e64 v245, 0, v244, s[74:75]\n"
    "v_or_b32 v38, v38, v245\n"
    "v_cndmask_b32_e64 v39, 0, v243, s[76:77]\n"
    "v_cndmask_b32_e64 v245, 0, v244, s[78:79]\n"
    "v_or_b32 v39, v39, v245\n"
    "v_cndmask_b32_e64 v40, 0, v243, s[80:81]\n"
    "v_cndmask_b32_e64 v245, 0, v244, s[82:83]\n"
    "s_waitcnt vmcnt(6)\n"
    "s_add_u32 m0, s44, 116224\n"
    "s_add_u32 s62, s10, s43\n"
    "s_addc_u32 s63, s11, 0\n"
    "global_load_lds_dwordx4 v6, s[62:63]\n"
    "s_lshl1_add_u32 s54, s33, s54\n"
    "s_lshl1_add_u32 s56, s34, s56\n"
    "s_mul_i32 s55, s54, 65792\n"
    "v_or_b32 v40, v40, v245\n"
    "v_cndmask_b32_e64 v41, 0, v243, s[84:85]\n"
    "v_cndmask_b32_e64 v245, 0, v244, s[86:87]\n"
    "v_or_b32 v41, v41, v245\n"
    "v_cndmask_b32_e64 v42, 0, v243, s[88:89]\n"
    "v_cndmask_b32_e64 v245, 0, v244, s[90:91]\n"
    "v_or_b32 v42, v42, v245\n"
    "v_cndmask_b32_e64 v43, 0, v243, s[92:93]\n"
    "s_waitcnt vmcnt(6)\n"
    "s_add_u32 m0, s45, 116224\n"
    "s_nop 0\n"
    "global_load_lds_dwordx4 v7, s[62:63]\n"
    "s_lshl_b32 s57, s56, 12\n"
    "s_add_u32 s55, s55, s57\n"
    "s_add_u32 s5, s55, 2621440\n"
    "v_cndmask_b32_e64 v245, 0, v244, s[94:95]\n"
    "v_or_b32 v43, v43, v245\n"
    "v_mov_b32 v64, 0\n"
    "v_mov_b32 v65, s38\n"
    "v_mov_b32 v86, 0\n"
    "v_mov_b32 v87, s38\n"
    "s_waitcnt vmcnt(6)\n"
    "s_barrier\n"
    "s_add_u32 m0, s46, 116224\n"
    "s_nop 0\n"
    "global_load_lds_dwordx4 v8, s[62:63]\n"
    "s_lshl4_add_u32 s54, s54, s56\n"
    "s_lshl_b32 s54, s54, 8\n"
    "s_add_u32 s0, s54, 327680\n"
    "s_waitcnt vmcnt(6)\n"
    "s_add_u32 m0, s47, 116224\n"
    "s_nop 0\n"
    "global_load_lds_dwordx4 v9, s[62:63]\n"
    "s_waitcnt vmcnt(6)\n"
    "s_sub_u32 s61, s3, 526336\n"
    "s_max_i32 s61, s61, s4\n"
    "s_add_u32 s62, s16, s61\n"
    "s_addc_u32 s63, s17, 0\n"
    "global_load_dwordx4 v[70:73], v2, s[62:63] sc1\n"
    "s_sub_u32 s61, s3, 394752\n"
    "s_max_i32 s61, s61, s4\n"
    "s_add_u32 s62, s16, s61\n"
    "s_addc_u32 s63, s17, 0\n"
    "global_load_dwordx4 v[74:77], v2, s[62:63] sc1\n"
    "s_sub_u32 s61, s3, 328960\n"
    "s_max_i32 s61, s61, s4\n"
    "s_add_u32 s62, s16, s61\n"
    "s_addc_u32 s63, s17, 0\n"
    "global_load_dwordx4 v[78:81], v2, s[62:63] sc1\n"
    "s_sub_u32 s61, s3, 263168\n"
    "s_max_i32 s61, s61, s4\n"
    "s_add_u32 s62, s16, s61\n"
    "s_addc_u32 s63, s17, 0\n"
    "global_load_dwordx4 v[82:85], v2, s[62:63] sc1\n"
    "s_mov_b32 exec_lo, 0xf000f\n"
    "s_mov_b32 exec_hi, 0xf000f\n"
    "global_load_dwordx2 v[86:87], v15, s[16:17] sc1\n"
    "s_mov_b64 exec, -1\n"
    "s_waitcnt vmcnt(5)\n"
    "s_waitcnt lgkmcnt(0)\n"
    "s_barrier\n"
    "ds_write_b32 v163, v106\n"
    "s_mov_b64 exec, 0xffff\n"
    "ds_write_b128 v152, v[148:151]\n"
    "s_mov_b64 exec, -1\n"
    "s_add_u32 m0, s1, 35840\n"
    "s_sub_u32 s61, s3, 460544\n"
    "s_max_i32 s61, s61, s4\n"
    "s_add_u32 s62, s16, s61\n"
    "s_addc_u32 s63, s17, 0\n"
    "global_load_lds_dwordx4 v2, s[62:63] sc1\n"
    "s_add_u32 m0, s1, 36864\n"
    "s_sub_u32 s61, s3, 197376\n"
    "s_max_i32 s61, s61, s4\n"
    "s_add_u32 s62, s16, s61\n"
    "s_addc_u32 s63, s17, 0\n"
    "global_load_lds_dwordx4 v2, s[62:63] sc1\n"
    "s_add_u32 m0, s1, 37888\n"
    "s_sub_u32 s61, s3, 131584\n"
    "s_max_i32 s61, s61, s4\n"
    "s_add_u32 s62, s16, s61\n"
    "s_addc_u32 s63, s17, 0\n"
    "global_load_lds_dwordx4 v2, s[62:63] sc1\n"
    "s_add_u32 m0, s1, 38912\n"
    "s_sub_u32 s61, s3, 65792\n"
    "s_max_i32 s61, s61, s4\n"
    "s_add_u32 s62, s16, s61\n"
    "s_addc_u32 s63, s17, 0\n"
    "global_load_lds_dwordx4 v2, s[62:63] sc1\n"
    "s_add_u32 m0, s1, 39936\n"
    "s_mov_b32 exec_lo, 0xf000f\n"
    "s_mov_b32 exec_hi, 0xf000f\n"
    "global_load_lds_dwordx4 v144, s[16:17] sc1\n"
    "s_mov_b64 exec, -1\n"
    "ds_read_b128 v[148:151], v147 offset:8192\n"
    "ds_read_b128 v[152:155], v147 offset:9216\n"
    "s_waitcnt lgkmcnt(0)\n"
    "v_add3_u32 v240, v148, v149, v150\n"
    "v_add_u32 v240, v240, v151\n"
    "v_add3_u32 v242, v152, v153, v154\n"
    "v_add_u32 v242, v242, v155\n"
    "v_mov_b32 v241, s38\n"
    "v_add_u32_dpp v240, v240, v240 row_ror:8 row_mask:0xf bank_mask:0xf bound_ctrl:1\n"
    "v_add_u32_dpp v242, v242, v242 row_ror:8 row_mask:0xf bank_mask:0xf bound_ctrl:1\n"
    "s_nop 0\n"
    "v_add_u32_dpp v240, v240, v240 row_ror:4 row_mask:0xf bank_mask:0xf bound_ctrl:1\n"
    "v_add_u32_dpp v242, v242, v242 row_ror:4 row_mask:0xf bank_mask:0xf bound_ctrl:1\n"
    "s_nop 0\n"
    "v_add_u32_dpp v240, v240, v240 row_ror:2 row_mask:0xf bank_mask:0xf bound_ctrl:1\n"
    "v_add_u32_dpp v242, v242, v242 row_ror:2 row_mask:0xf bank_mask:0xf bound_ctrl:1\n"
    "s_nop 0\n"
    "v_add_u32_dpp v240, v240, v240 row_ror:1 row_mask:0xf bank_mask:0xf bound_ctrl:1\n"
    "v_add_u32_dpp v242, v242, v242 row_ror:1 row_mask:0xf bank_mask:0xf bound_ctrl:1\n"
    "s_mov_b32 vcc_lo, 0xf0f0f0f0\n"
    "s_mov_b32 vcc_hi, 0xf0f0f0f0\n"
    "v_cndmask_b32 v240, v240, v242, vcc\n"
    "s_mov_b32 vcc_lo, 0xcccccccc\n"
    "s_mov_b32 vcc_hi, 0xcccccccc\n"
    "v_cndmask_b32 v240, v240, v241, vcc\n"
    "v_lshrrev_b32 v240, v2, v240\n"
    "s_add_u32 s62, s16, s5\n"
    "s_addc_u32 s63, s17, 0\n"
    "global_store_dwordx4 v2, v[148:151], s[62:63] offset:1024 sc1\n"
    "global_store_dwordx4 v2, v[152:155], s[62:63] offset:3072 sc1\n"
    "s_add_u32 s62, s16, s0\n"
    "s_addc_u32 s63, s17, 0\n"
    "global_store_short v10, v240, s[62:63] offset:128 sc1\n"
    "s_mov_b64 s[40:41], s[24:25]\n"
    "s_and_b32 s54, s37, 1\n"
    "s_lshl_b32 s54, s54, 2\n"
    "s_xor_b32 s54, s54, s30\n"
    "s_and_b32 s44, s54, 1\n"
    "s_lshr_b32 s45, s54, 1\n"
    "s_waitcnt vmcnt(8)\n"
    "v_add3_u32 v238, v70, v71, v72\n"
    "v_add3_u32 v238, v238, v73, v74\n"
    "v_add3_u32 v238, v238, v75, v76\n"
    "v_add3_u32 v238, v238, v77, v78\n"
    "v_add3_u32 v238, v238, v79, v80\n"
    "v_add3_u32 v238, v238, v81, v82\n"
    "v_add3_u32 v238, v238, v83, v84\n"
    "v_add_u32 v238, v238, v85\n"
    "v_sub_u32 v238, v238, v86\n"
    "s_nop 1\n"
    "v_add_u32_dpp v238, v238, v238 row_ror:8 row_mask:0xf bank_mask:0xf bound_ctrl:1\n"
    "s_nop 1\n"
    "v_add_u32_dpp v238, v238, v238 row_ror:4 row_mask:0xf bank_mask:0xf bound_ctrl:1\n"
    "s_nop 1\n"
    "v_add_u32_dpp v238, v238, v238 row_ror:2 row_mask:0xf bank_mask:0xf bound_ctrl:1\n"
    "s_nop 1\n"
    "v_add_u32_dpp v238, v238, v238 row_ror:1 row_mask:0xf bank_mask:0xf bound_ctrl:1\n"
    "v_bitop3_b32 v238, v238, v87, s38 bitop3:0xf6\n"
    "v_cmp_ne_u32 vcc, 0, v238\n"
    "s_cbranch_vccnz .Lkr3\n"
    ".Lkb4:\n"
    "s_cmp_lg_u32 s51, 0\n"
    "s_cbranch_scc1 .Lkf5\n"
    ".Lkfb6:\n"
    "s_waitcnt vmcnt(3)\n"
    "s_setprio 0\n"
    "s_branch .LattH\n"
    ".LM1:\n"
    "s_setprio 1\n"
    "v_bfe_u32 v238, v1, 2, 2\n"
    "v_lshlrev_b32 v5, 7, v238\n"
    "v_lshrrev_b32 v238, 1, v238\n"
    "v_lshl_add_u32 v5, v238, 6, v5\n"
    "v_bfe_u32 v238, v1, 4, 1\n"
    "v_lshl_add_u32 v5, v238, 5, v5\n"
    "v_and_b32 v238, 3, v1\n"
    "v_lshl_add_u32 v5, v238, 3, v5\n"
    "v_lshrrev_b32 v238, 5, v1\n"
    "v_lshl_add_u32 v5, v238, 10, v5\n"
    "s_and_b32 s56, s30, 1\n"
    "s_lshl_b32 s57, s56, 6\n"
    "v_xor_b32 v5, s57, v5\n"
    "s_lshr_b32 s58, s39, 1\n"
    "s_mul_i32 s59, s58, 8448\n"
    "s_add_u32 s59, s59, 65536\n"
    "v_lshrrev_b32 v238, 5, v1\n"
    "v_lshlrev_b32 v238, 4, v238\n"
    "v_add3_u32 v248, v2, v238, s59\n"
    "s_waitcnt lgkmcnt(0)\n"
    "s_barrier\n"
    "s_add_u32 s38, s38, 1\n"
    "s_barrier\n"
    "global_load_dwordx4 v[64:67], v115, s[8:9] offset:0\n"
    "global_load_dwordx4 v[68:71], v115, s[8:9] offset:1024\n"
    "global_load_dwordx4 v[72:75], v116, s[8:9] offset:0\n"
    "global_load_dwordx4 v[76:79], v116, s[8:9] offset:1024\n"
    "global_load_dwordx4 v[80:83], v117, s[8:9] offset:0\n"
    "global_load_dwordx4 v[84:87], v117, s[8:9] offset:1024\n"
    "v_add_u32 v251, 65536, v5\n"
    "v_add_u32 v253, 512, v251\n"
    "s_lshl_b32 s58, s39, 11\n"
    "s_add_u32 s58, s58, 133120\n"
    "v_add_u32 v252, s58, v2\n"
    "s_lshl2_add_u32 s57, s34, s39\n"
    "s_lshl_b32 s56, s33, 1\n"
    "v_and_b32 v241, 3, v1\n"
    "v_lshlrev_b32 v241, 2, v241\n"
    "s_mov_b32 s54, 0x7651\n"
    "v_bfe_u32 v241, s54, v241, 4\n"
    "s_sub_i32 s56, s56, 8\n"
    "v_add_u32 v241, s56, v241\n"
    "v_cmp_gt_i32 vcc, 0, v241\n"
    "s_and_b32 s51, vcc_lo, 15\n"
    "s_and_b32 s54, s51, 14\n"
    "s_add_u32 s51, s51, s54\n"
    "v_max_i32 v241, 0, v241\n"
    "s_lshr_b32 s54, s57, 1\n"
    "s_lshl_b32 s54, s54, 8\n"
    "s_and_b32 s57, s57, 1\n"
    "s_lshl3_add_u32 s54, s57, s54\n"
    "s_add_u32 s54, s54, 327680\n"
    "v_lshl_add_u32 v15, v241, 12, s54\n"
    "v_lshrrev_b32 v241, 4, v1\n"
    "v_lshl_add_u32 v15, v241, 5, v15\n"
    "v_lshlrev_b32 v10, 1, v1\n"
    "v_and_b32 v13, 0x70, v2\n"
    "v_lshrrev_b32 v243, 3, v1\n"
    "v_lshl_add_u32 v12, v243, 10, v13\n"
    "v_lshlrev_b32 v243, 2, v1\n"
    "v_and_b32 v243, 64, v243\n"
    "v_xor_b32 v11, v12, v243\n"
    "v_lshrrev_b32 v243, 4, v1\n"
    "v_mul_u32_u24 v243, 24576, v243\n"
    "v_and_b32 v244, 0xf0, v2\n"
    "v_add_u32 v14, v243, v244\n"
    "v_lshl_add_u32 v233, s30, 13, v2\n"
    "v_xor_b32 v234, 0x8000, v233\n"
    "v_lshl_add_u32 v243, s30, 10, v2\n"
    "v_add_u32 v235, 133120, v243\n"
    "v_xor_b32 v236, 4096, v243\n"
    "v_add_u32 v236, 133120, v236\n"
    "v_mov_b32 v244, 141312\n"
    "v_add_u32 v243, 155648, v243\n"
    "v_add_u32 v237, 3072, v233\n"
    "s_mov_b32 vcc_lo, 0xffff0000\n"
    "s_mov_b32 vcc_hi, 0xffff0000\n"
    "v_cndmask_b32 v250, v243, v244, vcc\n"
    "v_cndmask_b32 v237, v237, v244, vcc\n"
    "v_mov_b32 v249, v237\n"
    "s_lshl2_add_u32 s54, s32, s39\n"
    "v_bfe_u32 v229, v1, 2, 2\n"
    "v_lshl_add_u32 v229, s54, 2, v229\n"
    "v_mul_u32_u24 v229, 3584, v229\n"
    "s_min_u32 s54, s31, 6\n"
    "s_lshl_b32 s54, s54, 9\n"
    "v_and_b32 v243, 3, v1\n"
    "v_lshlrev_b32 v243, 7, v243\n"
    "v_add3_u32 v229, v229, v243, s54\n"
    "s_and_b32 s54, s37, 1\n"
    "s_lshl_b32 s54, s54, 2\n"
    "s_xor_b32 s54, s54, s30\n"
    "s_and_b32 s55, s54, 1\n"
    "s_lshr_b32 s56, s54, 1\n"
    "s_lshl_b32 s56, s56, 14\n"
    "s_add_u32 s56, s56, 65536\n"
    "v_bfe_u32 v238, v1, 2, 2\n"
    "v_lshlrev_b32 v3, 7, v238\n"
    "v_lshrrev_b32 v238, 1, v238\n"
    "v_lshl_add_u32 v3, v238, 6, v3\n"
    "v_bfe_u32 v238, v1, 4, 1\n"
    "v_lshl_add_u32 v3, v238, 5, v3\n"
    "v_and_b32 v238, 3, v1\n"
    "v_lshl_add_u32 v3, v238, 3, v3\n"
    "v_lshrrev_b32 v238, 5, v1\n"
    "v_lshl_add_u32 v3, v238, 9, v3\n"
    "v_add_u32 v3, s56, v3\n"
    "s_lshl_b32 s6, s37, 7\n"
    "s_lshr_b32 s7, s32, 3\n"
    "s_lshl4_add_u32 s7, s7, s30\n"
    "s_lshl_b32 s7, s7, 10\n"
    "s_lshl3_add_u32 s12, s36, s30\n"
    "s_lshl_b32 s12, s12, 13\n"
    "s_add_u32 s12, s12, s6\n"
    "s_add_u32 s18, s18, s6\n"
    "s_addc_u32 s19, s19, 0\n"
    "s_add_u32 s20, s20, s6\n"
    "s_addc_u32 s21, s21, 0\n"
    "s_add_u32 s8, s8, s12\n"
    "s_addc_u32 s9, s9, 0\n"
    "s_lshr_b32 s54, s30, 1\n"
    "s_lshl_b32 s54, s54, 12\n"
    "s_lshl2_add_u32 s54, s6, s54\n"
    "s_and_b32 s55, s30, 1\n"
    "s_lshl_b32 s55, s55, 8\n"
    "s_add_u32 s54, s54, s55\n"
    "s_add_u32 s15, s54, 12288\n"
    "s_lshr_b32 s54, s30, 2\n"
    "s_lshl1_add_u32 s54, s33, s54\n"
    "s_lshl2_add_u32 s55, s34, s39\n"
    "s_mul_i32 s56, s54, 33024\n"
    "s_lshl_b32 s57, s55, 10\n"
    "s_add_u32 s56, s56, s57\n"
    "s_add_u32 s13, s56, 7340032\n"
    "s_lshl_b32 s54, s54, 5\n"
    "s_add_u32 s54, s54, s55\n"
    "s_lshl_b32 s54, s54, 7\n"
    "s_add_u32 s14, s54, 589824\n"
    "s_lshl1_add_u32 s4, s57, 2621440\n"
    "s_mul_i32 s3, s33, 131584\n"
    "s_add_u32 s3, s3, s4\n"
    "s_lshl_b32 s1, s30, 13\n"
    "s_and_b32 s56, s30, 1\n"
    "s_lshr_b32 s54, s39, 1\n"
    "s_lshl1_add_u32 s54, s33, s54\n"
    "s_lshl1_add_u32 s56, s34, s56\n"
    "s_mul_i32 s55, s54, 65792\n"
    "s_lshl_b32 s57, s56, 12\n"
    "s_add_u32 s55, s55, s57\n"
    "s_add_u32 s5, s55, 2621440\n"
    "s_lshl4_add_u32 s54, s54, s56\n"
    "s_lshl_b32 s54, s54, 8\n"
    "s_add_u32 s0, s54, 327680\n"
    "s_mov_b64 s[40:41], s[26:27]\n"
    "s_and_b32 s54, s37, 1\n"
    "s_lshl_b32 s54, s54, 2\n"
    "s_xor_b32 s54, s54, s30\n"
    "s_and_b32 s44, s54, 1\n"
    "s_lshr_b32 s45, s54, 1\n"
    "s_waitcnt vmcnt(8)\n"
    "v_dot2c_f32_bf16 v120, s49, v56\n"
    "v_dot2c_f32_bf16 v121, v56, v56\n"
    "v_lshlrev_b32 v16, 16, v56\n"
    "v_and_b32 v17, s50, v56\n"
    "v_dot2c_f32_bf16 v120, s49, v57\n"
    "v_dot2c_f32_bf16 v121, v57, v57\n"
    "v_lshlrev_b32 v18, 16, v57\n"
    "v_and_b32 v19, s50, v57\n"
    "v_dot2c_f32_bf16 v120, s49, v58\n"
    "v_dot2c_f32_bf16 v121, v58, v58\n"
    "v_lshlrev_b32 v20, 16, v58\n"
    "v_and_b32 v21, s50, v58\n"
    "v_dot2c_f32_bf16 v120, s49, v59\n"
    "v_dot2c_f32_bf16 v121, v59, v59\n"
    "v_lshlrev_b32 v22, 16, v59\n"
    "v_and_b32 v23, s50, v59\n"
    "s_waitcnt vmcnt(7)\n"
    "v_dot2c_f32_bf16 v122, s49, v60\n"
    "v_dot2c_f32_bf16 v123, v60, v60\n"
    "v_lshlrev_b32 v24, 16, v60\n"
    "v_and_b32 v25, s50, v60\n"
    "v_dot2c_f32_bf16 v122, s49, v61\n"
    "v_dot2c_f32_bf16 v123, v61, v61\n"
    "v_lshlrev_b32 v26, 16, v61\n"
    "v_and_b32 v27, s50, v61\n"
    "v_dot2c_f32_bf16 v122, s49, v62\n"
    "v_dot2c_f32_bf16 v123, v62, v62\n"
    "v_lshlrev_b32 v28, 16, v62\n"
    "v_and_b32 v29, s50, v62\n"
    "v_dot2c_f32_bf16 v122, s49, v63\n"
    "v_dot2c_f32_bf16 v123, v63, v63\n"
    "v_lshlrev_b32 v30, 16, v63\n"
    "v_and_b32 v31, s50, v63\n"
    "s_waitcnt vmcnt(5)\n"
    "v_dot2c_f32_bf16 v124, s49, v64\n"
    "v_dot2c_f32_bf16 v125, v64, v64\n"
    "v_lshlrev_b32 v32, 16, v64\n"
    "v_and_b32 v33, s50, v64\n"
    "v_dot2c_f32_bf16 v124, s49, v65\n"
    "v_dot2c_f32_bf16 v125, v65, v65\n"
    "v_lshlrev_b32 v34, 16, v65\n"
    "v_and_b32 v35, s50, v65\n"
    "v_dot2c_f32_bf16 v124, s49, v66\n"
    "v_dot2c_f32_bf16 v125, v66, v66\n"
    "v_lshlrev_b32 v36, 16, v66\n"
    "v_and_b32 v37, s50, v66\n"
    "v_dot2c_f32_bf16 v124, s49, v67\n"
    "v_dot2c_f32_bf16 v125, v67, v67\n"
    "v_lshlrev_b32 v38, 16, v67\n"
    "v_and_b32 v39, s50, v67\n"
    "s_waitcnt vmcnt(4)\n"
    "v_dot2c_f32_bf16 v126, s49, v68\n"
    "v_dot2c_f32_bf16 v127, v68, v68\n"
    "v_lshlrev_b32 v40, 16, v68\n"
    "v_and_b32 v41, s50, v68\n"
    "v_dot2c_f32_bf16 v126, s49, v69\n"
    "v_dot2c_f32_bf16 v127, v69, v69\n"
    "v_lshlrev_b32 v42, 16, v69\n"
    "v_and_b32 v43, s50, v69\n"
    "v_dot2c_f32_bf16 v126, s49, v70\n"
    "v_dot2c_f32_bf16 v127, v70, v70\n"
    "v_lshlrev_b32 v44, 16, v70\n"
    "v_and_b32 v45, s50, v70\n"
    "v_dot2c_f32_bf16 v126, s49, v71\n"
    "v_dot2c_f32_bf16 v127, v71, v71\n"
    "v_lshlrev_b32 v46, 16, v71\n"
    "v_and_b32 v47, s50, v71\n"
    "s_waitcnt vmcnt(3)\n"
    "v_dot2c_f32_bf16 v128, s49, v72\n"
    "v_dot2c_f32_bf16 v129, v72, v72\n"
    "v_lshlrev_b32 v48, 16, v72\n"
    "v_and_b32 v49, s50, v72\n"
    "v_dot2c_f32_bf16 v128, s49, v73\n"
    "v_dot2c_f32_bf16 v129, v73, v73\n"
    "v_lshlrev_b32 v50, 16, v73\n"
    "v_and_b32 v51, s50, v73\n"
    "v_dot2c_f32_bf16 v128, s49, v74\n"
    "v_dot2c_f32_bf16 v129, v74, v74\n"
    "v_lshlrev_b32 v52, 16, v74\n"
    "v_and_b32 v53, s50, v74\n"
    "v_dot2c_f32_bf16 v128, s49, v75\n"
    "v_dot2c_f32_bf16 v129, v75, v75\n"
    "v_lshlrev_b32 v54, 16, v75\n"
    "v_and_b32 v55, s50, v75\n"
    "s_waitcnt vmcnt(2)\n"
    "v_dot2c_f32_bf16 v130, s49, v76\n"
    "v_dot2c_f32_bf16 v131, v76, v76\n"
    "v_lshlrev_b32 v184, 16, v76\n"
    "v_and_b32 v185, s50, v76\n"
    "v_dot2c_f32_bf16 v130, s49, v77\n"
    "v_dot2c_f32_bf16 v131, v77, v77\n"
    "v_lshlrev_b32 v186, 16, v77\n"
    "v_and_b32 v187, s50, v77\n"
    "v_dot2c_f32_bf16 v130, s49, v78\n"
    "v_dot2c_f32_bf16 v131, v78, v78\n"
    "v_lshlrev_b32 v188, 16, v78\n"
    "v_and_b32 v189, s50, v78\n"
    "v_dot2c_f32_bf16 v130, s49, v79\n"
    "v_dot2c_f32_bf16 v131, v79, v79\n"
    "v_lshlrev_b32 v190, 16, v79\n"
    "v_and_b32 v191, s50, v79\n"
    "s_waitcnt vmcnt(1)\n"
    "v_dot2c_f32_bf16 v132, s49, v80\n"
    "v_dot2c_f32_bf16 v133, v80, v80\n"
    "v_lshlrev_b32 v192, 16, v80\n"
    "v_and_b32 v193, s50, v80\n"
    "v_dot2c_f32_bf16 v132, s49, v81\n"
    "v_dot2c_f32_bf16 v133, v81, v81\n"
    "v_lshlrev_b32 v194, 16, v81\n"
    "v_and_b32 v195, s50, v81\n"
    "v_dot2c_f32_bf16 v132, s49, v82\n"
    "v_dot2c_f32_bf16 v133, v82, v82\n"
    "v_lshlrev_b32 v196, 16, v82\n"
    "v_and_b32 v197, s50, v82\n"
    "v_dot2c_f32_bf16 v132, s49, v83\n"
    "v_dot2c_f32_bf16 v133, v83, v83\n"
    "v_lshlrev_b32 v198, 16, v83\n"
    "v_and_b32 v199, s50, v83\n"
    "s_waitcnt vmcnt(0)\n"
    "v_dot2c_f32_bf16 v134, s49, v84\n"
    "v_dot2c_f32_bf16 v135, v84, v84\n"
    "v_lshlrev_b32 v200, 16, v84\n"
    "v_and_b32 v201, s50, v84\n"
    "v_dot2c_f32_bf16 v134, s49, v85\n"
    "v_dot2c_f32_bf16 v135, v85, v85\n"
    "v_lshlrev_b32 v202, 16, v85\n"
    "v_and_b32 v203, s50, v85\n"
    "v_dot2c_f32_bf16 v134, s49, v86\n"
    "v_dot2c_f32_bf16 v135, v86, v86\n"
    "v_lshlrev_b32 v204, 16, v86\n"
    "v_and_b32 v205, s50, v86\n"
    "v_dot2c_f32_bf16 v134, s49, v87\n"
    "v_dot2c_f32_bf16 v135, v87, v87\n"
    "v_lshlrev_b32 v206, 16, v87\n"
    "v_and_b32 v207, s50, v87\n"
    "v_permlane32_swap_b32 v120, v128\n"
    "v_permlane32_swap_b32 v121, v129\n"
    "v_permlane32_swap_b32 v122, v130\n"
    "v_permlane32_swap_b32 v123, v131\n"
    "v_permlane32_swap_b32 v124, v132\n"
    "v_permlane32_swap_b32 v125, v133\n"
    "v_permlane32_swap_b32 v126, v134\n"
    "v_permlane32_swap_b32 v127, v135\n"
    "v_pk_add_f32 v[120:121], v[120:121], v[128:129]\n"
    "v_pk_add_f32 v[122:123], v[122:123], v[130:131]\n"
    "v_pk_add_f32 v[124:125], v[124:125], v[132:133]\n"
    "v_pk_add_f32 v[126:127], v[126:127], v[134:135]\n"
    "s_nop 0\n"
    "v_permlane16_swap_b32 v120, v124\n"
    "v_permlane16_swap_b32 v121, v125\n"
    "v_permlane16_swap_b32 v122, v126\n"
    "v_permlane16_swap_b32 v123, v127\n"
    "v_pk_add_f32 v[120:121], v[120:121], v[124:125]\n"
    "v_pk_add_f32 v[122:123], v[122:123], v[126:127]\n"
    "s_nop 0\n"
    "v_add_f32_dpp v120, v120, v120 row_ror:8 row_mask:0xf bank_mask:0xf bound_ctrl:1\n"
    "v_add_f32_dpp v121, v121, v121 row_ror:8 row_mask:0xf bank_mask:0xf bound_ctrl:1\n"
    "v_add_f32_dpp v122, v122, v122 row_ror:8 row_mask:0xf bank_mask:0xf bound_ctrl:1\n"
    "v_add_f32_dpp v123, v123, v123 row_ror:8 row_mask:0xf bank_mask:0xf bound_ctrl:1\n"
    "v_add_f32_dpp v120, v120, v120 row_ror:4 row_mask:0xf bank_mask:0xf bound_ctrl:1\n"
    "v_add_f32_dpp v121, v121, v121 row_ror:4 row_mask:0xf bank_mask:0xf bound_ctrl:1\n"
    "v_add_f32_dpp v122, v122, v122 row_ror:4 row_mask:0xf bank_mask:0xf bound_ctrl:1\n"
    "v_add_f32_dpp v123, v123, v123 row_ror:4 row_mask:0xf bank_mask:0xf bound_ctrl:1\n"
    "v_add_f32_dpp v120, v120, v120 row_ror:2 row_mask:0xf bank_mask:0xf bound_ctrl:1\n"
    "v_add_f32_dpp v121, v121, v121 row_ror:2 row_mask:0xf bank_mask:0xf bound_ctrl:1\n"
    "v_add_f32_dpp v122, v122, v122 row_ror:2 row_mask:0xf bank_mask:0xf bound_ctrl:1\n"
    "v_add_f32_dpp v123, v123, v123 row_ror:2 row_mask:0xf bank_mask:0xf bound_ctrl:1\n"
    "v_add_f32_dpp v120, v120, v120 row_ror:1 row_mask:0xf bank_mask:0xf bound_ctrl:1\n"
    "v_add_f32_dpp v121, v121, v121 row_ror:1 row_mask:0xf bank_mask:0xf bound_ctrl:1\n"
    "v_add_f32_dpp v122, v122, v122 row_ror:1 row_mask:0xf bank_mask:0xf bound_ctrl:1\n"
    "v_add_f32_dpp v123, v123, v123 row_ror:1 row_mask:0xf bank_mask:0xf bound_ctrl:1\n"
    "v_pk_mul_f32 v[120:121], v[120:121], s[98:99] op_sel_hi:[1,0]\n"
    "v_pk_mul_f32 v[122:123], v[122:123], s[98:99] op_sel_hi:[1,0]\n"
    "v_fma_f32 v121, -v120, v120, v121\n"
    "v_fma_f32 v123, -v122, v122, v123\n"
    "v_max_f32 v121, 0, v121\n"
    "v_max_f32 v123, 0, v123\n"
    "v_add_f32 v121, 0x3727c5ac, v121\n"
    "v_add_f32 v123, 0x3727c5ac, v123\n"
    "v_rsq_f32 v121, v121\n"
    "v_rsq_f32 v123, v123\n"
    "v_mul_f32_e64 v120, -v120, v121\n"
    "v_mul_f32_e64 v122, -v122, v123\n"
    "v_readlane_b32 s69, v121, 0\n"
    "v_readlane_b32 s68, v120, 0\n"
    "v_readlane_b32 s71, v123, 0\n"
    "v_readlane_b32 s70, v122, 0\n"
    "v_readlane_b32 s73, v121, 16\n"
    "v_readlane_b32 s72, v120, 16\n"
    "v_readlane_b32 s75, v123, 16\n"
    "v_readlane_b32 s74, v122, 16\n"
    "v_readlane_b32 s77, v121, 32\n"
    "v_readlane_b32 s76, v120, 32\n"
    "v_readlane_b32 s79, v123, 32\n"
    "v_readlane_b32 s78, v122, 32\n"
    "v_readlane_b32 s81, v121, 48\n"
    "v_readlane_b32 s80, v120, 48\n"
    "v_readlane_b32 s83, v123, 48\n"
    "v_readlane_b32 s82, v122, 48\n"
    "v_pk_fma_f32 v[16:17], v[16:17], s[68:69], s[68:69] op_sel:[0,1,0] op_sel_hi:[1,1,0]\n"
    "v_pk_fma_f32 v[18:19], v[18:19], s[68:69], s[68:69] op_sel:[0,1,0] op_sel_hi:[1,1,0]\n"
    "v_pk_fma_f32 v[20:21], v[20:21], s[68:69], s[68:69] op_sel:[0,1,0] op_sel_hi:[1,1,0]\n"
    "v_pk_fma_f32 v[22:23], v[22:23], s[68:69], s[68:69] op_sel:[0,1,0] op_sel_hi:[1,1,0]\n"
    "v_pk_fma_f32 v[24:25], v[24:25], s[70:71], s[70:71] op_sel:[0,1,0] op_sel_hi:[1,1,0]\n"
    "v_pk_fma_f32 v[26:27], v[26:27], s[70:71], s[70:71] op_sel:[0,1,0] op_sel_hi:[1,1,0]\n"
    "v_pk_fma_f32 v[28:29], v[28:29], s[70:71], s[70:71] op_sel:[0,1,0] op_sel_hi:[1,1,0]\n"
    "v_pk_fma_f32 v[30:31], v[30:31], s[70:71], s[70:71] op_sel:[0,1,0] op_sel_hi:[1,1,0]\n"
    "v_pk_fma_f32 v[32:33], v[32:33], s[72:73], s[72:73] op_sel:[0,1,0] op_sel_hi:[1,1,0]\n"
    "v_pk_fma_f32 v[34:35], v[34:35], s[72:73], s[72:73] op_sel:[0,1,0] op_sel_hi:[1,1,0]\n"
    "v_pk_fma_f32 v[36:37], v[36:37], s[72:73], s[72:73] op_sel:[0,1,0] op_sel_hi:[1,1,0]\n"
    "v_pk_fma_f32 v[38:39], v[38:39], s[72:73], s[72:73] op_sel:[0,1,0] op_sel_hi:[1,1,0]\n"
    "v_pk_fma_f32 v[40:41], v[40:41], s[74:75], s[74:75] op_sel:[0,1,0] op_sel_hi:[1,1,0]\n"
    "v_pk_fma_f32 v[42:43], v[42:43], s[74:75], s[74:75] op_sel:[0,1,0] op_sel_hi:[1,1,0]\n"
    "v_pk_fma_f32 v[44:45], v[44:45], s[74:75], s[74:75] op_sel:[0,1,0] op_sel_hi:[1,1,0]\n"
    "v_pk_fma_f32 v[46:47], v[46:47], s[74:75], s[74:75] op_sel:[0,1,0] op_sel_hi:[1,1,0]\n"
    "v_pk_fma_f32 v[48:49], v[48:49], s[76:77], s[76:77] op_sel:[0,1,0] op_sel_hi:[1,1,0]\n"
    "v_pk_fma_f32 v[50:51], v[50:51], s[76:77], s[76:77] op_sel:[0,1,0] op_sel_hi:[1,1,0]\n"
    "v_pk_fma_f32 v[52:53], v[52:53], s[76:77], s[76:77] op_sel:[0,1,0] op_sel_hi:[1,1,0]\n"
    "v_pk_fma_f32 v[54:55], v[54:55], s[76:77], s[76:77] op_sel:[0,1,0] op_sel_hi:[1,1,0]\n"
    "v_pk_fma_f32 v[184:185], v[184:185], s[78:79], s[78:79] op_sel:[0,1,0] op_sel_hi:[1,1,0]\n"
    "v_pk_fma_f32 v[186:187], v[186:187], s[78:79], s[78:79] op_sel:[0,1,0] op_sel_hi:[1,1,0]\n"
    "v_pk_fma_f32 v[188:189], v[188:189], s[78:79], s[78:79] op_sel:[0,1,0] op_sel_hi:[1,1,0]\n"
    "v_pk_fma_f32 v[190:191], v[190:191], s[78:79], s[78:79] op_sel:[0,1,0] op_sel_hi:[1,1,0]\n"
    "v_pk_fma_f32 v[192:193], v[192:193], s[80:81], s[80:81] op_sel:[0,1,0] op_sel_hi:[1,1,0]\n"
    "v_pk_fma_f32 v[194:195], v[194:195], s[80:81], s[80:81] op_sel:[0,1,0] op_sel_hi:[1,1,0]\n"
    "v_pk_fma_f32 v[196:197], v[196:197], s[80:81], s[80:81] op_sel:[0,1,0] op_sel_hi:[1,1,0]\n"
    "v_pk_fma_f32 v[198:199], v[198:199], s[80:81], s[80:81] op_sel:[0,1,0] op_sel_hi:[1,1,0]\n"
    "v_pk_fma_f32 v[200:201], v[200:201], s[82:83], s[82:83] op_sel:[0,1,0] op_sel_hi:[1,1,0]\n"
    "v_pk_fma_f32 v[202:203], v[202:203], s[82:83], s[82:83] op_sel:[0,1,0] op_sel_hi:[1,1,0]\n"
    "v_pk_fma_f32 v[204:205], v[204:205], s[82:83], s[82:83] op_sel:[0,1,0] op_sel_hi:[1,1,0]\n"
    "v_pk_fma_f32 v[206:207], v[206:207], s[82:83], s[82:83] op_sel:[0,1,0] op_sel_hi:[1,1,0]\n"
    "global_load_dwordx4 v[208:211], v230, s[16:17] sc1\n"
    "v_add_u32 v231, 24576, v230\n"
    "global_load_dwordx4 v[212:215], v231, s[16:17] sc1\n"
    "v_add_u32 v230, 24576, v231\n"
    "global_load_dwordx4 v[216:219], v230, s[16:17] sc1\n"
    "v_add_u32 v231, 24576, v230\n"
    "global_load_dwordx4 v[220:223], v231, s[16:17] sc1\n"
    "v_add_u32 v230, -73728, v231\n"
    "s_barrier\n"
    "ds_read_b64_tr_b16 v[56:57], v5 offset:0\n"
    "ds_read_b64_tr_b16 v[58:59], v5 offset:512\n"
    "ds_read_b64_tr_b16 v[60:61], v5 offset:2048\n"
    "ds_read_b64_tr_b16 v[62:63], v5 offset:2560\n"
    "ds_read_b64_tr_b16 v[64:65], v5 offset:4096\n"
    "ds_read_b64_tr_b16 v[66:67], v5 offset:4608\n"
    "ds_read_b64_tr_b16 v[68:69], v5 offset:6144\n"
    "ds_read_b64_tr_b16 v[70:71], v5 offset:6656\n"
    "ds_read_b64_tr_b16 v[72:73], v5 offset:8192\n"
    "ds_read_b64_tr_b16 v[74:75], v5 offset:8704\n"
    "ds_read_b64_tr_b16 v[76:77], v5 offset:10240\n"
    "ds_read_b64_tr_b16 v[78:79], v5 offset:10752\n"
    "ds_read_b64_tr_b16 v[80:81], v5 offset:12288\n"
    "ds_read_b64_tr_b16 v[82:83], v5 offset:12800\n"
    "ds_read_b64_tr_b16 v[84:85], v5 offset:14336\n"
    "ds_read_b64_tr_b16 v[86:87], v5 offset:14848\n"
    "ds_read_b64_tr_b16 v[88:89], v5 offset:16384\n"
    "ds_read_b64_tr_b16 v[90:91], v5 offset:16896\n"
    "ds_read_b64_tr_b16 v[92:93], v5 offset:18432\n"
    "ds_read_b64_tr_b16 v[94:95], v5 offset:18944\n"
    "ds_read_b64_tr_b16 v[96:97], v5 offset:20480\n"
    "ds_read_b64_tr_b16 v[98:99], v5 offset:20992\n"
    "ds_read_b64_tr_b16 v[100:101], v5 offset:22528\n"
    "ds_read_b64_tr_b16 v[102:103], v5 offset:23040\n"
    "ds_read_b64_tr_b16 v[104:105], v5 offset:24576\n"
    "ds_read_b64_tr_b16 v[106:107], v5 offset:25088\n"
    "ds_read_b64_tr_b16 v[108:109], v5 offset:26624\n"
    "ds_read_b64_tr_b16 v[110:111], v5 offset:27136\n"
    "ds_read_b64_tr_b16 v[112:113], v5 offset:28672\n"
    "ds_read_b64_tr_b16 v[114:115], v5 offset:29184\n"
    "ds_read_b64_tr_b16 v[116:117], v5 offset:30720\n"
    "ds_read_b64_tr_b16 v[118:119], v5 offset:31232\n"
    "ds_read_b64_tr_b16 v[120:121], v5 offset:32768\n"
    "ds_read_b64_tr_b16 v[122:123], v5 offset:33280\n"
    "ds_read_b64_tr_b16 v[124:125], v5 offset:34816\n"
    "ds_read_b64_tr_b16 v[126:127], v5 offset:35328\n"
    "ds_read_b64_tr_b16 v[128:129], v5 offset:36864\n"
    "ds_read_b64_tr_b16 v[130:131], v5 offset:37376\n"
    "ds_read_b64_tr_b16 v[132:133], v5 offset:38912\n"
    "ds_read_b64_tr_b16 v[134:135], v5 offset:39424\n"
    "ds_read_b64_tr_b16 v[136:137], v5 offset:40960\n"
    "ds_read_b64_tr_b16 v[138:139], v5 offset:41472\n"
    "ds_read_b64_tr_b16 v[140:141], v5 offset:43008\n"
    "ds_read_b64_tr_b16 v[142:143], v5 offset:43520\n"
    "ds_read_b64_tr_b16 v[144:145], v5 offset:45056\n"
    "ds_read_b64_tr_b16 v[146:147], v5 offset:45568\n"
    "ds_read_b64_tr_b16 v[148:149], v5 offset:47104\n"
    "ds_read_b64_tr_b16 v[150:151], v5 offset:47616\n"
    "ds_read_b64_tr_b16 v[152:153], v5 offset:49152\n"
    "ds_read_b64_tr_b16 v[154:155], v5 offset:49664\n"
    "ds_read_b64_tr_b16 v[156:157], v5 offset:51200\n"
    "ds_read_b64_tr_b16 v[158:159], v5 offset:51712\n"
    "ds_read_b64_tr_b16 v[160:161], v5 offset:53248\n"
    "ds_read_b64_tr_b16 v[162:163], v5 offset:53760\n"
    "ds_read_b64_tr_b16 v[164:165], v5 offset:55296\n"
    "ds_read_b64_tr_b16 v[166:167], v5 offset:55808\n"
    "ds_read_b64_tr_b16 v[168:169], v5 offset:57344\n"
    "ds_read_b64_tr_b16 v[170:171], v5 offset:57856\n"
    "ds_read_b64_tr_b16 v[172:173], v5 offset:59392\n"
    "ds_read_b64_tr_b16 v[174:175], v5 offset:59904\n"
    "ds_read_b64_tr_b16 v[176:177], v5 offset:61440\n"
    "ds_read_b64_tr_b16 v[178:179], v5 offset:61952\n"
    "ds_read_b64_tr_b16 v[180:181], v5 offset:63488\n"
    "ds_read_b64_tr_b16 v[182:183], v5 offset:64000\n"
    "s_waitcnt vmcnt(0)\n"
    "v_xor_b32 v239, s38, v209\n"
    "v_bitop3_b32 v239, v239, v211, s38 bitop3:0xf6\n"
    "v_bitop3_b32 v239, v239, v213, s38 bitop3:0xf6\n"
    "v_bitop3_b32 v239, v239, v215, s38 bitop3:0xf6\n"
    "v_bitop3_b32 v239, v239, v217, s38 bitop3:0xf6\n"
    "v_bitop3_b32 v239, v239, v219, s38 bitop3:0xf6\n"
    "v_bitop3_b32 v239, v239, v221, s38 bitop3:0xf6\n"
    "v_bitop3_b32 v239, v239, v223, s38 bitop3:0xf6\n"
    "v_cmp_ne_u32 vcc, 0, v239\n"
    "s_cbranch_vccnz .Lmr13\n"
    ".Lmb14:\n"
    "v_add_f32 v208, v208, v212\n"
    "v_add_f32 v210, v210, v214\n"
    "v_add_f32 v216, v216, v220\n"
    "v_add_f32 v218, v218, v222\n"
    "v_add_f32 v208, v208, v216\n"
    "v_add_f32 v210, v210, v218\n"
    "v_cvt_pk_bf16_f32 v209, v208, v210\n"
    "v_lshlrev_b32 v208, 16, v209\n"
    "v_and_b32 v209, s50, v209\n"
    "v_pk_add_f32 v[208:209], v[208:209], 1.0 op_sel_hi:[1,0]\n"
    "v_cvt_pk_bf16_f32 v209, v208, v209\n"
    "v_lshlrev_b32 v208, 16, v209\n"
    "v_and_b32 v209, s50, v209\n"
    "ds_write_b64 v232, v[208:209] offset:0\n"
    "s_waitcnt lgkmcnt(0)\n"
    "s_barrier\n"
    "ds_read_b128 v[208:211], v228 offset:0\n"
    "ds_read_b128 v[216:219], v228 offset:2048\n"
    "ds_read_b128 v[212:215], v228 offset:1024\n"
    "ds_read_b128 v[220:223], v228 offset:3072\n"
    "s_waitcnt lgkmcnt(2)\n"
    "v_pk_fma_f32 v[16:17], v[16:17], v[208:209], v[216:217]\n"
    "v_pk_fma_f32 v[18:19], v[18:19], v[210:211], v[218:219]\n"
    "s_waitcnt lgkmcnt(0)\n"
    "v_pk_fma_f32 v[20:21], v[20:21], v[212:213], v[220:221]\n"
    "v_pk_fma_f32 v[22:23], v[22:23], v[214:215], v[222:223]\n"
    "v_cvt_pk_bf16_f32 v16, v16, v17\n"
    "v_cvt_pk_bf16_f32 v17, v18, v19\n"
    "v_cvt_pk_bf16_f32 v18, v20, v21\n"
    "v_cvt_pk_bf16_f32 v19, v22, v23\n"
    "ds_write_b128 v224, v[16:19] offset:0\n"
    "v_pk_fma_f32 v[24:25], v[24:25], v[208:209], v[216:217]\n"
    "v_pk_fma_f32 v[26:27], v[26:27], v[210:211], v[218:219]\n"
    "v_pk_fma_f32 v[28:29], v[28:29], v[212:213], v[220:221]\n"
    "v_pk_fma_f32 v[30:31], v[30:31], v[214:215], v[222:223]\n"
    "v_cvt_pk_bf16_f32 v24, v24, v25\n"
    "v_cvt_pk_bf16_f32 v25, v26, v27\n"
    "v_cvt_pk_bf16_f32 v26, v28, v29\n"
    "v_cvt_pk_bf16_f32 v27, v30, v31\n"
    "ds_write_b128 v224, v[24:27] offset:16\n"
    "v_pk_fma_f32 v[32:33], v[32:33], v[208:209], v[216:217]\n"
    "v_pk_fma_f32 v[34:35], v[34:35], v[210:211], v[218:219]\n"
    "v_pk_fma_f32 v[36:37], v[36:37], v[212:213], v[220:221]\n"
    "v_pk_fma_f32 v[38:39], v[38:39], v[214:215], v[222:223]\n"
    "v_cvt_pk_bf16_f32 v32, v32, v33\n"
    "v_cvt_pk_bf16_f32 v33, v34, v35\n"
    "v_cvt_pk_bf16_f32 v34, v36, v37\n"
    "v_cvt_pk_bf16_f32 v35, v38, v39\n"
    "ds_write_b128 v225, v[32:35] offset:0\n"
    "v_pk_fma_f32 v[40:41], v[40:41], v[208:209], v[216:217]\n"
    "v_pk_fma_f32 v[42:43], v[42:43], v[210:211], v[218:219]\n"
    "v_pk_fma_f32 v[44:45], v[44:45], v[212:213], v[220:221]\n"
    "v_pk_fma_f32 v[46:47], v[46:47], v[214:215], v[222:223]\n"
    "v_cvt_pk_bf16_f32 v40, v40, v41\n"
    "v_cvt_pk_bf16_f32 v41, v42, v43\n"
    "v_cvt_pk_bf16_f32 v42, v44, v45\n"
    "v_cvt_pk_bf16_f32 v43, v46, v47\n"
    "ds_write_b128 v225, v[40:43] offset:16\n"
    "v_pk_fma_f32 v[48:49], v[48:49], v[208:209], v[216:217]\n"
    "v_pk_fma_f32 v[50:51], v[50:51], v[210:211], v[218:219]\n"
    "v_pk_fma_f32 v[52:53], v[52:53], v[212:213], v[220:221]\n"
    "v_pk_fma_f32 v[54:55], v[54:55], v[214:215], v[222:223]\n"
    "v_cvt_pk_bf16_f32 v48, v48, v49\n"
    "v_cvt_pk_bf16_f32 v49, v50, v51\n"
    "v_cvt_pk_bf16_f32 v50, v52, v53\n"
    "v_cvt_pk_bf16_f32 v51, v54, v55\n"
    "ds_write_b128 v226, v[48:51] offset:0\n"
    "v_pk_fma_f32 v[184:185], v[184:185], v[208:209], v[216:217]\n"
    "v_pk_fma_f32 v[186:187], v[186:187], v[210:211], v[218:219]\n"
    "v_pk_fma_f32 v[188:189], v[188:189], v[212:213], v[220:221]\n"
    "v_pk_fma_f32 v[190:191], v[190:191], v[214:215], v[222:223]\n"
    "v_cvt_pk_bf16_f32 v184, v184, v185\n"
    "v_cvt_pk_bf16_f32 v185, v186, v187\n"
    "v_cvt_pk_bf16_f32 v186, v188, v189\n"
    "v_cvt_pk_bf16_f32 v187, v190, v191\n"
    "ds_write_b128 v226, v[184:187] offset:16\n"
    "v_pk_fma_f32 v[192:193], v[192:193], v[208:209], v[216:217]\n"
    "v_pk_fma_f32 v[194:195], v[194:195], v[210:211], v[218:219]\n"
    "v_pk_fma_f32 v[196:197], v[196:197], v[212:213], v[220:221]\n"
    "v_pk_fma_f32 v[198:199], v[198:199], v[214:215], v[222:223]\n"
    "v_cvt_pk_bf16_f32 v192, v192, v193\n"
    "v_cvt_pk_bf16_f32 v193, v194, v195\n"
    "v_cvt_pk_bf16_f32 v194, v196, v197\n"
    "v_cvt_pk_bf16_f32 v195, v198, v199\n"
    "ds_write_b128 v227, v[192:195] offset:0\n"
    "v_pk_fma_f32 v[200:201], v[200:201], v[208:209], v[216:217]\n"
    "v_pk_fma_f32 v[202:203], v[202:203], v[210:211], v[218:219]\n"
    "v_pk_fma_f32 v[204:205], v[204:205], v[212:213], v[220:221]\n"
    "v_pk_fma_f32 v[206:207], v[206:207], v[214:215], v[222:223]\n"
    "v_cvt_pk_bf16_f32 v200, v200, v201\n"
    "v_cvt_pk_bf16_f32 v201, v202, v203\n"
    "v_cvt_pk_bf16_f32 v202, v204, v205\n"
    "v_cvt_pk_bf16_f32 v203, v206, v207\n"
    "ds_write_b128 v227, v[200:203] offset:16\n"
    "s_waitcnt lgkmcnt(0)\n"
    "s_barrier\n"
    "ds_read_b128 v[16:19], v248 offset:0\n"
    "ds_read_b128 v[20:23], v248 offset:1056\n"
    "ds_read_b128 v[24:27], v248 offset:2112\n"
    "ds_read_b128 v[28:31], v248 offset:3168\n"
    "ds_read_b128 v[32:35], v248 offset:4224\n"
    "ds_read_b128 v[36:39], v248 offset:5280\n"
    "ds_read_b128 v[40:43], v248 offset:6336\n"
    "ds_read_b128 v[44:47], v248 offset:7392\n"
    "ds_read_b128 v[48:51], v248 offset:16896\n"
    "ds_read_b128 v[52:55], v248 offset:17952\n"
    "s_waitcnt lgkmcnt(9)\n"
    "v_mfma_f32_32x32x16_bf16 v[184:199], v[56:59], v[16:19], 0\n"
    "ds_read_b128 v[56:59], v248 offset:19008\n"
    "s_mov_b32 s98, 0x3eb8aa3b\n"
    "s_mov_b32 s64, 0x0\n"
    "s_mov_b32 s65, 0xf\n"
    "s_mov_b32 s66, 0x1\n"
    "s_waitcnt lgkmcnt(9)\n"
    "v_mfma_f32_32x32x16_bf16 v[184:199], v[60:63], v[20:23], v[184:199]\n"
    "ds_read_b128 v[60:63], v248 offset:20064\n"
    "s_mov_b32 s67, 0x1f\n"
    "s_mov_b32 s68, 0x3\n"
    "s_mov_b32 s69, 0x3f\n"
    "s_mov_b32 s70, 0x7\n"
    "s_waitcnt lgkmcnt(9)\n"
    "v_mfma_f32_32x32x16_bf16 v[184:199], v[64:67], v[24:27], v[184:199]\n"
    "ds_read_b128 v[64:67], v248 offset:21120\n"
    "s_mov_b32 s71, 0x7f\n"
    "s_mov_b32 s72, 0xff\n"
    "s_mov_b32 s73, 0xfff\n"
    "s_mov_b32 s74, 0x1ff\n"
    "s_waitcnt lgkmcnt(9)\n"
    "v_mfma_f32_32x32x16_bf16 v[184:199], v[68:71], v[28:31], v[184:199]\n"
    "ds_read_b128 v[68:71], v248 offset:22176\n"
    "s_mov_b32 s75, 0x1fff\n"
    "s_mov_b32 s76, 0x3ff\n"
    "s_mov_b32 s77, 0x3fff\n"
    "s_mov_b32 s78, 0x7ff\n"
    "s_waitcnt lgkmcnt(9)\n"
    "v_mfma_f32_32x32x16_bf16 v[184:199], v[72:75], v[32:35], v[184:199]\n"
    "ds_read_b128 v[72:75], v248 offset:23232\n"
    "s_mov_b32 s79, 0x7fff\n"
    "s_mov_b32 s80, 0xffff\n"
    "s_mov_b32 s81, 0xfffff\n"
    "s_mov_b32 s82, 0x1ffff\n"
    "s_waitcnt lgkmcnt(9)\n"
    "v_mfma_f32_32x32x16_bf16 v[184:199], v[76:79], v[36:39], v[184:199]\n"
    "ds_read_b128 v[76:79], v248 offset:24288\n"
    "s_mov_b32 s83, 0x1fffff\n"
    "s_mov_b32 s84, 0x3ffff\n"
    "s_mov_b32 s85, 0x3fffff\n"
    "s_mov_b32 s86, 0x7ffff\n"
    "s_waitcnt lgkmcnt(9)\n"
    "v_mfma_f32_32x32x16_bf16 v[184:199], v[80:83], v[40:43], v[184:199]\n"
    "ds_read_b128 v[80:83], v248 offset:33792\n"
    "s_mov_b32 s87, 0x7fffff\n"
    "s_mov_b32 s88, 0xffffff\n"
    "s_mov_b32 s89, 0xfffffff\n"
    "s_mov_b32 s90, 0x1ffffff\n"
    "s_waitcnt lgkmcnt(9)\n"
    "v_mfma_f32_32x32x16_bf16 v[184:199], v[84:87], v[44:47], v[184:199]\n"
    "ds_read_b128 v[84:87], v248 offset:34848\n"
    "s_mov_b32 s91, 0x1fffffff\n"
    "s_mov_b32 s92, 0x3ffffff\n"
    "s_mov_b32 s93, 0x3fffffff\n"
    "s_mov_b32 s94, 0x7ffffff\n"
    "s_waitcnt lgkmcnt(9)\n"
    "v_mfma_f32_32x32x16_bf16 v[184:199], v[88:91], v[48:51], v[184:199]\n"
    "ds_read_b128 v[88:91], v248 offset:35904\n"
    "s_mov_b32 s95, 0x7fffffff\n"
    "s_waitcnt lgkmcnt(9)\n"
    "v_mfma_f32_32x32x16_bf16 v[184:199], v[92:95], v[52:55], v[184:199]\n"
    "ds_read_b128 v[92:95], v248 offset:36960\n"
    "s_waitcnt lgkmcnt(9)\n"
    "v_mfma_f32_32x32x16_bf16 v[184:199], v[96:99], v[56:59], v[184:199]\n"
    "ds_read_b128 v[96:99], v248 offset:38016\n"
    "s_waitcnt lgkmcnt(9)\n"
    "v_mfma_f32_32x32x16_bf16 v[184:199], v[100:103], v[60:63], v[184:199]\n"
    "ds_read_b128 v[100:103], v248 offset:39072\n"
    "s_waitcnt lgkmcnt(9)\n"
    "v_mfma_f32_32x32x16_bf16 v[184:199], v[104:107], v[64:67], v[184:199]\n"
    "ds_read_b128 v[104:107], v248 offset:40128\n"
    "s_waitcnt lgkmcnt(9)\n"
    "v_mfma_f32_32x32x16_bf16 v[184:199], v[108:111], v[68:71], v[184:199]\n"
    "ds_read_b128 v[108:111], v248 offset:41184\n"
    "s_waitcnt lgkmcnt(9)\n"
    "v_mfma_f32_32x32x16_bf16 v[184:199], v[112:115], v[72:75], v[184:199]\n"
    "ds_read_b128 v[112:115], v248 offset:50688\n"
    "s_waitcnt lgkmcnt(9)\n"
    "v_mfma_f32_32x32x16_bf16 v[184:199], v[116:119], v[76:79], v[184:199]\n"
    "ds_read_b128 v[116:119], v248 offset:51744\n"
    "s_waitcnt lgkmcnt(9)\n"
    "v_mfma_f32_32x32x16_bf16 v[184:199], v[120:123], v[80:83], v[184:199]\n"
    "ds_read_b128 v[120:123], v248 offset:52800\n"
    "s_waitcnt lgkmcnt(9)\n"
    "v_mfma_f32_32x32x16_bf16 v[184:199], v[124:127], v[84:87], v[184:199]\n"
    "ds_read_b128 v[124:127], v248 offset:53856\n"
    "s_waitcnt lgkmcnt(9)\n"
    "v_mfma_f32_32x32x16_bf16 v[184:199], v[128:131], v[88:91], v[184:199]\n"
    "ds_read_b128 v[128:131], v248 offset:54912\n"
    "s_waitcnt lgkmcnt(9)\n"
    "v_mfma_f32_32x32x16_bf16 v[184:199], v[132:135], v[92:95], v[184:199]\n"
    "ds_read_b128 v[132:135], v248 offset:55968\n"
    "s_waitcnt lgkmcnt(9)\n"
    "v_mfma_f32_32x32x16_bf16 v[184:199], v[136:139], v[96:99], v[184:199]\n"
    "ds_read_b128 v[136:139], v248 offset:57024\n"
    "s_waitcnt lgkmcnt(9)\n"
    "v_mfma_f32_32x32x16_bf16 v[184:199], v[140:143], v[100:103], v[184:199]\n"
    "ds_read_b128 v[140:143], v248 offset:58080\n"
    "s_waitcnt lgkmcnt(9)\n"
    "v_mfma_f32_32x32x16_bf16 v[184:199], v[144:147], v[104:107], v[184:199]\n"
    "s_waitcnt lgkmcnt(8)\n"
    "v_mfma_f32_32x32x16_bf16 v[184:199], v[148:151], v[108:111], v[184:199]\n"
    "s_waitcnt lgkmcnt(7)\n"
    "v_mfma_f32_32x32x16_bf16 v[184:199], v[152:155], v[112:115], v[184:199]\n"
    "s_waitcnt lgkmcnt(6)\n"
    "v_mfma_f32_32x32x16_bf16 v[184:199], v[156:159], v[116:119], v[184:199]\n"
    "s_waitcnt lgkmcnt(5)\n"
    "v_mfma_f32_32x32x16_bf16 v[184:199], v[160:163], v[120:123], v[184:199]\n"
    "s_waitcnt lgkmcnt(0)\n"
    "s_barrier\n"
    "ds_read_b64_tr_b16 v[144:145], v5 offset:0\n"
    "ds_read_b64_tr_b16 v[146:147], v5 offset:512\n"
    "v_mfma_f32_32x32x16_bf16 v[184:199], v[164:167], v[124:127], v[184:199]\n"
    "ds_read_b64_tr_b16 v[148:149], v5 offset:2048\n"
    "ds_read_b64_tr_b16 v[150:151], v5 offset:2560\n"
    "v_mfma_f32_32x32x16_bf16 v[184:199], v[168:171], v[128:131], v[184:199]\n"
    "ds_read_b64_tr_b16 v[152:153], v5 offset:4096\n"
    "ds_read_b64_tr_b16 v[154:155], v5 offset:4608\n"
    "v_mfma_f32_32x32x16_bf16 v[184:199], v[172:175], v[132:135], v[184:199]\n"
    "ds_read_b64_tr_b16 v[156:157], v5 offset:6144\n"
    "ds_read_b64_tr_b16 v[158:159], v5 offset:6656\n"
    "v_mfma_f32_32x32x16_bf16 v[184:199], v[176:179], v[136:139], v[184:199]\n"
    "ds_read_b64_tr_b16 v[160:161], v5 offset:8192\n"
    "ds_read_b64_tr_b16 v[162:163], v5 offset:8704\n"
    "v_mfma_f32_32x32x16_bf16 v[184:199], v[180:183], v[140:143], v[184:199]\n"
    "ds_read_b64_tr_b16 v[164:165], v5 offset:10240\n"
    "ds_read_b64_tr_b16 v[166:167], v5 offset:10752\n"
    "s_waitcnt lgkmcnt(10)\n"
    "v_mfma_f32_32x32x16_bf16 v[200:215], v[16:19], v[144:147], 0\n"
    "ds_read_b64_tr_b16 v[168:169], v5 offset:12288\n"
    "ds_read_b64_tr_b16 v[170:171], v5 offset:12800\n"
    "s_waitcnt lgkmcnt(10)\n"
    "v_mfma_f32_32x32x16_bf16 v[200:215], v[20:23], v[148:151], v[200:215]\n"
    "ds_read_b64_tr_b16 v[172:173], v5 offset:14336\n"
    "ds_read_b64_tr_b16 v[174:175], v5 offset:14848\n"
    "s_waitcnt lgkmcnt(10)\n"
    "v_mfma_f32_32x32x16_bf16 v[200:215], v[24:27], v[152:155], v[200:215]\n"
    "ds_read_b64_tr_b16 v[176:177], v5 offset:16384\n"
    "ds_read_b64_tr_b16 v[178:179], v5 offset:16896\n"
    "v_cvt_pk_bf16_f32 v216, v184, v185\n"
    "v_cvt_pk_bf16_f32 v217, v186, v187\n"
    "v_cvt_pk_bf16_f32 v218, v188, v189\n"
    "s_waitcnt lgkmcnt(10)\n"
    "v_mfma_f32_32x32x16_bf16 v[200:215], v[28:31], v[156:159], v[200:215]\n"
    "ds_read_b64_tr_b16 v[180:181], v5 offset:18432\n"
    "ds_read_b64_tr_b16 v[182:183], v5 offset:18944\n"
    "v_cvt_pk_bf16_f32 v219, v190, v191\n"
    "v_cvt_pk_bf16_f32 v220, v192, v193\n"
    "v_cvt_pk_bf16_f32 v221, v194, v195\n"
    "s_waitcnt lgkmcnt(10)\n"
    "v_mfma_f32_32x32x16_bf16 v[200:215], v[32:35], v[160:163], v[200:215]\n"
    "ds_read_b64_tr_b16 v[144:145], v5 offset:20480\n"
    "ds_read_b64_tr_b16 v[146:147], v5 offset:20992\n"
    "v_cvt_pk_bf16_f32 v222, v196, v197\n"
    "v_cvt_pk_bf16_f32 v223, v198, v199\n"
    "v_add3_u32 v240, v216, v217, v218\n"
    "s_waitcnt lgkmcnt(10)\n"
    "v_mfma_f32_32x32x16_bf16 v[200:215], v[36:39], v[164:167], v[200:215]\n"
    "ds_read_b64_tr_b16 v[148:149], v5 offset:22528\n"
    "ds_read_b64_tr_b16 v[150:151], v5 offset:23040\n"
    "v_add_u32 v240, v240, v219\n"
    "v_add3_u32 v242, v220, v221, v222\n"
    "v_add_u32 v242, v242, v223\n"
    "s_waitcnt lgkmcnt(10)\n"
    "v_mfma_f32_32x32x16_bf16 v[200:215], v[40:43], v[168:171], v[200:215]\n"
    "ds_read_b64_tr_b16 v[152:153], v5 offset:24576\n"
    "ds_read_b64_tr_b16 v[154:155], v5 offset:25088\n"
    "v_mov_b32 v241, s38\n"
    "v_add_u32_dpp v240, v240, v240 row_ror:8 row_mask:0xf bank_mask:0xf bound_ctrl:1\n"
    "v_add_u32_dpp v242, v242, v242 row_ror:8 row_mask:0xf bank_mask:0xf bound_ctrl:1\n"
    "s_waitcnt lgkmcnt(10)\n"
    "v_mfma_f32_32x32x16_bf16 v[200:215], v[44:47], v[172:175], v[200:215]\n"
    "ds_read_b64_tr_b16 v[156:157], v5 offset:26624\n"
    "ds_read_b64_tr_b16 v[158:159], v5 offset:27136\n"
    "v_add_u32_dpp v240, v240, v240 row_ror:4 row_mask:0xf bank_mask:0xf bound_ctrl:1\n"
    "v_add_u32_dpp v242, v242, v242 row_ror:4 row_mask:0xf bank_mask:0xf bound_ctrl:1\n"
    "s_nop 0\n"
    "v_add_u32_dpp v240, v240, v240 row_ror:2 row_mask:0xf bank_mask:0xf bound_ctrl:1\n"
    "s_waitcnt lgkmcnt(10)\n"
    "v_mfma_f32_32x32x16_bf16 v[200:215], v[48:51], v[176:179], v[200:215]\n"
    "ds_read_b64_tr_b16 v[160:161], v5 offset:28672\n"
    "ds_read_b64_tr_b16 v[162:163], v5 offset:29184\n"
    "v_add_u32_dpp v242, v242, v242 row_ror:2 row_mask:0xf bank_mask:0xf bound_ctrl:1\n"
    "v_add_u32_dpp v240, v240, v240 row_ror:1 row_mask:0xf bank_mask:0xf bound_ctrl:1\n"
    "s_nop 0\n"
    "v_add_u32_dpp v242, v242, v242 row_ror:1 row_mask:0xf bank_mask:0xf bound_ctrl:1\n"
    "s_waitcnt lgkmcnt(10)\n"
    "v_mfma_f32_32x32x16_bf16 v[200:215], v[52:55], v[180:183], v[200:215]\n"
    "ds_read_b64_tr_b16 v[164:165], v5 offset:30720\n"
    "ds_read_b64_tr_b16 v[166:167], v5 offset:31232\n"
    "s_mov_b32 vcc_lo, 0xf0f0f0f0\n"
    "s_mov_b32 vcc_hi, 0xf0f0f0f0\n"
    "v_cndmask_b32 v240, v240, v242, vcc\n"
    "s_mov_b32 vcc_lo, 0xcccccccc\n"
    "s_mov_b32 vcc_hi, 0xcccccccc\n"
    "s_waitcnt lgkmcnt(10)\n"
    "v_mfma_f32_32x32x16_bf16 v[200:215], v[56:59], v[144:147], v[200:215]\n"
    "s_barrier\n"
    "ds_read_b64_tr_b16 v[168:169], v5 offset:32768\n"
    "ds_read_b64_tr_b16 v[170:171], v5 offset:33280\n"
    "v_cndmask_b32 v240, v240, v241, vcc\n"
    "v_lshrrev_b32 v240, v2, v240\n"
    "s_waitcnt lgkmcnt(10)\n"
    "v_mfma_f32_32x32x16_bf16 v[200:215], v[60:63], v[148:151], v[200:215]\n"
    "ds_read_b64_tr_b16 v[172:173], v5 offset:34816\n"
    "ds_read_b64_tr_b16 v[174:175], v5 offset:35328\n"
    "s_add_u32 s62, s16, s5\n"
    "s_addc_u32 s63, s17, 0\n"
    "global_store_dwordx4 v2, v[216:219], s[62:63] sc1\n"
    "global_store_dwordx4 v2, v[220:223], s[62:63] offset:2048 sc1\n"
    "s_add_u32 s62, s16, s0\n"
    "s_addc_u32 s63, s17, 0\n"
    "global_store_short v10, v240, s[62:63] offset:0 sc1\n"
    "s_waitcnt lgkmcnt(10)\n"
    "v_mfma_f32_32x32x16_bf16 v[200:215], v[64:67], v[152:155], v[200:215]\n"
    "ds_read_b64_tr_b16 v[176:177], v5 offset:36864\n"
    "ds_read_b64_tr_b16 v[178:179], v5 offset:37376\n"
    "s_waitcnt lgkmcnt(10)\n"
    "v_mfma_f32_32x32x16_bf16 v[200:215], v[68:71], v[156:159], v[200:215]\n"
    "ds_read_b64_tr_b16 v[180:181], v5 offset:38912\n"
    "ds_read_b64_tr_b16 v[182:183], v5 offset:39424\n"
    "s_waitcnt lgkmcnt(10)\n"
    "v_mfma_f32_32x32x16_bf16 v[200:215], v[72:75], v[160:163], v[200:215]\n"
    "ds_read_b64_tr_b16 v[144:145], v5 offset:40960\n"
    "ds_read_b64_tr_b16 v[146:147], v5 offset:41472\n"
    "s_waitcnt lgkmcnt(10)\n"
    "v_mfma_f32_32x32x16_bf16 v[200:215], v[76:79], v[164:167], v[200:215]\n"
    "ds_read_b64_tr_b16 v[148:149], v5 offset:43008\n"
    "ds_read_b64_tr_b16 v[150:151], v5 offset:43520\n"
    "s_waitcnt lgkmcnt(10)\n"
    "v_mfma_f32_32x32x16_bf16 v[200:215], v[80:83], v[168:171], v[200:215]\n"
    "ds_read_b64_tr_b16 v[152:153], v5 offset:45056\n"
    "ds_read_b64_tr_b16 v[154:155], v5 offset:45568\n"
    "s_waitcnt lgkmcnt(10)\n"
    "v_mfma_f32_32x32x16_bf16 v[200:215], v[84:87], v[172:175], v[200:215]\n"
    "ds_read_b64_tr_b16 v[156:157], v5 offset:47104\n"
    "ds_read_b64_tr_b16 v[158:159], v5 offset:47616\n"
    "s_waitcnt lgkmcnt(10)\n"
    "v_mfma_f32_32x32x16_bf16 v[200:215], v[88:91], v[176:179], v[200:215]\n"
    "ds_read_b64_tr_b16 v[160:161], v5 offset:49152\n"
    "ds_read_b64_tr_b16 v[162:163], v5 offset:49664\n"
    "s_waitcnt lgkmcnt(10)\n"
    "v_mfma_f32_32x32x16_bf16 v[200:215], v[92:95], v[180:183], v[200:215]\n"
    "ds_read_b64_tr_b16 v[164:165], v5 offset:51200\n"
    "ds_read_b64_tr_b16 v[166:167], v5 offset:51712\n"
    "s_waitcnt lgkmcnt(10)\n"
    "v_mfma_f32_32x32x16_bf16 v[200:215], v[96:99], v[144:147], v[200:215]\n"
    "ds_read_b64_tr_b16 v[168:169], v5 offset:53248\n"
    "ds_read_b64_tr_b16 v[170:171], v5 offset:53760\n"
    "s_waitcnt lgkmcnt(10)\n"
    "v_mfma_f32_32x32x16_bf16 v[200:215], v[100:103], v[148:151], v[200:215]\n"
    "ds_read_b64_tr_b16 v[172:173], v5 offset:55296\n"
    "ds_read_b64_tr_b16 v[174:175], v5 offset:55808\n"
    "s_waitcnt lgkmcnt(10)\n"
    "v_mfma_f32_32x32x16_bf16 v[200:215], v[104:107], v[152:155], v[200:215]\n"
    "ds_read_b64_tr_b16 v[176:177], v5 offset:57344\n"
    "ds_read_b64_tr_b16 v[178:179], v5 offset:57856\n"
    "s_waitcnt lgkmcnt(10)\n"
    "v_mfma_f32_32x32x16_bf16 v[200:215], v[108:111], v[156:159], v[200:215]\n"
    "ds_read_b64_tr_b16 v[180:181], v5 offset:59392\n"
    "ds_read_b64_tr_b16 v[182:183], v5 offset:59904\n"
    "s_waitcnt lgkmcnt(10)\n"
    "v_mfma_f32_32x32x16_bf16 v[200:215], v[112:115], v[160:163], v[200:215]\n"
    "ds_read_b64_tr_b16 v[144:145], v5 offset:61440\n"
    "ds_read_b64_tr_b16 v[146:147], v5 offset:61952\n"
    "s_waitcnt lgkmcnt(10)\n"
    "v_mfma_f32_32x32x16_bf16 v[200:215], v[116:119], v[164:167], v[200:215]\n"
    "ds_read_b64_tr_b16 v[148:149], v5 offset:63488\n"
    "ds_read_b64_tr_b16 v[150:151], v5 offset:64000\n"
    "s_waitcnt lgkmcnt(10)\n"
    "v_mfma_f32_32x32x16_bf16 v[200:215], v[120:123], v[168:171], v[200:215]\n"
    "s_barrier\n"
    "ds_read_b64_tr_b16 v[152:153], v251 offset:0\n"
    "ds_read_b64_tr_b16 v[154:155], v253 offset:0\n"
    "s_waitcnt lgkmcnt(10)\n"
    "v_mfma_f32_32x32x16_bf16 v[200:215], v[124:127], v[172:175], v[200:215]\n"
    "ds_read_b64_tr_b16 v[156:157], v251 offset:2048\n"
    "ds_read_b64_tr_b16 v[158:159], v253 offset:2048\n"
    "s_waitcnt lgkmcnt(10)\n"
    "v_mfma_f32_32x32x16_bf16 v[200:215], v[128:131], v[176:179], v[200:215]\n"
    "ds_read_b64_tr_b16 v[160:161], v251 offset:4096\n"
    "ds_read_b64_tr_b16 v[162:163], v253 offset:4096\n"
    "s_waitcnt lgkmcnt(10)\n"
    "v_mfma_f32_32x32x16_bf16 v[200:215], v[132:135], v[180:183], v[200:215]\n"
    "ds_read_b64_tr_b16 v[164:165], v251 offset:6144\n"
    "ds_read_b64_tr_b16 v[166:167], v253 offset:6144\n"
    "s_waitcnt lgkmcnt(10)\n"
    "v_mfma_f32_32x32x16_bf16 v[200:215], v[136:139], v[144:147], v[200:215]\n"
    "ds_read_b64_tr_b16 v[168:169], v251 offset:8192\n"
    "ds_read_b64_tr_b16 v[170:171], v253 offset:8192\n"
    "s_waitcnt lgkmcnt(10)\n"
    "v_mfma_f32_32x32x16_bf16 v[200:215], v[140:143], v[148:151], v[200:215]\n"
    "ds_read_b64_tr_b16 v[172:173], v251 offset:10240\n"
    "ds_read_b64_tr_b16 v[174:175], v253 offset:10240\n"
    "s_waitcnt lgkmcnt(10)\n"
    "v_mfma_f32_32x32x16_bf16 v[184:199], v[152:155], v[16:19], 0\n"
    "ds_read_b64_tr_b16 v[176:177], v251 offset:12288\n"
    "ds_read_b64_tr_b16 v[178:179], v253 offset:12288\n"
    "s_waitcnt lgkmcnt(10)\n"
    "v_mfma_f32_32x32x16_bf16 v[184:199], v[156:159], v[20:23], v[184:199]\n"
    "ds_read_b64_tr_b16 v[180:181], v251 offset:14336\n"
    "ds_read_b64_tr_b16 v[182:183], v253 offset:14336\n"
    "s_waitcnt lgkmcnt(10)\n"
    "v_mfma_f32_32x32x16_bf16 v[184:199], v[160:163], v[24:27], v[184:199]\n"
    "ds_read_b64_tr_b16 v[144:145], v251 offset:16896\n"
    "ds_read_b64_tr_b16 v[146:147], v253 offset:16896\n"
    "v_cvt_pk_bf16_f32 v200, v200, v201\n"
    "v_cvt_pk_bf16_f32 v201, v202, v203\n"
    "v_cvt_pk_bf16_f32 v202, v204, v205\n"
    "s_waitcnt lgkmcnt(10)\n"
    "v_mfma_f32_32x32x16_bf16 v[184:199], v[164:167], v[28:31], v[184:199]\n"
    "ds_read_b64_tr_b16 v[148:149], v251 offset:18944\n"
    "ds_read_b64_tr_b16 v[150:151], v253 offset:18944\n"
    "v_cvt_pk_bf16_f32 v203, v206, v207\n"
    "v_cvt_pk_bf16_f32 v204, v208, v209\n"
    "v_cvt_pk_bf16_f32 v205, v210, v211\n"
    "s_waitcnt lgkmcnt(10)\n"
    "v_mfma_f32_32x32x16_bf16 v[184:199], v[168:171], v[32:35], v[184:199]\n"
    "ds_read_b64_tr_b16 v[152:153], v251 offset:20992\n"
    "ds_read_b64_tr_b16 v[154:155], v253 offset:20992\n"
    "v_cvt_pk_bf16_f32 v206, v212, v213\n"
    "v_cvt_pk_bf16_f32 v207, v214, v215\n"
    "s_waitcnt lgkmcnt(10)\n"
    "v_mfma_f32_32x32x16_bf16 v[184:199], v[172:175], v[36:39], v[184:199]\n"
    "ds_read_b64_tr_b16 v[156:157], v251 offset:23040\n"
    "ds_read_b64_tr_b16 v[158:159], v253 offset:23040\n"
    "v_permlane16_swap_b32 v200, v204\n"
    "v_permlane16_swap_b32 v201, v205\n"
    "s_waitcnt lgkmcnt(10)\n"
    "v_mfma_f32_32x32x16_bf16 v[184:199], v[176:179], v[40:43], v[184:199]\n"
    "ds_read_b64_tr_b16 v[160:161], v251 offset:25088\n"
    "ds_read_b64_tr_b16 v[162:163], v253 offset:25088\n"
    "v_permlane16_swap_b32 v202, v206\n"
    "v_permlane16_swap_b32 v203, v207\n"
    "s_waitcnt lgkmcnt(10)\n"
    "v_mfma_f32_32x32x16_bf16 v[184:199], v[180:183], v[44:47], v[184:199]\n"
    "ds_read_b64_tr_b16 v[164:165], v251 offset:27136\n"
    "ds_read_b64_tr_b16 v[166:167], v253 offset:27136\n"
    "ds_write_b128 v252, v[200:203] offset:22528\n"
    "s_waitcnt lgkmcnt(11)\n"
    "v_mfma_f32_32x32x16_bf16 v[184:199], v[144:147], v[48:51], v[184:199]\n"
    "ds_read_b64_tr_b16 v[168:169], v251 offset:29184\n"
    "ds_read_b64_tr_b16 v[170:171], v253 offset:29184\n"
    "ds_write_b128 v252, v[204:207] offset:23552\n"
    "s_waitcnt lgkmcnt(12)\n"
    "v_mfma_f32_32x32x16_bf16 v[184:199], v[148:151], v[52:55], v[184:199]\n"
    "ds_read_b64_tr_b16 v[172:173], v251 offset:31232\n"
    "ds_read_b64_tr_b16 v[174:175], v253 offset:31232\n"
    "s_waitcnt lgkmcnt(12)\n"
    "v_mfma_f32_32x32x16_bf16 v[184:199], v[152:155], v[56:59], v[184:199]\n"
    "s_waitcnt lgkmcnt(2)\n"
    "s_barrier\n"
    "ds_read_b64_tr_b16 v[176:177], v251 offset:33792\n"
    "ds_read_b64_tr_b16 v[178:179], v253 offset:33792\n"
    "v_mfma_f32_32x32x16_bf16 v[184:199], v[156:159], v[60:63], v[184:199]\n"
    "ds_read_b64_tr_b16 v[180:181], v251 offset:35840\n"
    "ds_read_b64_tr_b16 v[182:183], v253 offset:35840\n"
    "ds_write_b128 v252, v[216:219] offset:14336\n"
    "v_mfma_f32_32x32x16_bf16 v[184:199], v[160:163], v[64:67], v[184:199]\n"
    "ds_read_b64_tr_b16 v[144:145], v251 offset:37888\n"
    "ds_read_b64_tr_b16 v[146:147], v253 offset:37888\n"
    "ds_write_b128 v252, v[220:223] offset:15360\n"
    "v_mfma_f32_32x32x16_bf16 v[184:199], v[164:167], v[68:71], v[184:199]\n"
    "ds_read_b64_tr_b16 v[148:149], v251 offset:39936\n"
    "ds_read_b64_tr_b16 v[150:151], v253 offset:39936\n"
    "v_mov_b32 v243, 0xffff\n"
    "v_mov_b32 v244, s50\n"
    "v_mfma_f32_32x32x16_bf16 v[184:199], v[168:171], v[72:75], v[184:199]\n"
    "ds_read_b64_tr_b16 v[152:153], v251 offset:41984\n"
    "ds_read_b64_tr_b16 v[154:155], v253 offset:41984\n"
    "v_cndmask_b32_e64 v36, 0, v243, s[64:65]\n"
    "v_cndmask_b32_e64 v245, 0, v244, s[66:67]\n"
    "s_waitcnt lgkmcnt(12)\n"
    "v_mfma_f32_32x32x16_bf16 v[184:199], v[172:175], v[76:79], v[184:199]\n"
    "ds_read_b64_tr_b16 v[156:157], v251 offset:44032\n"
    "ds_read_b64_tr_b16 v[158:159], v253 offset:44032\n"
    "v_or_b32 v36, v36, v245\n"
    "v_cndmask_b32_e64 v37, 0, v243, s[68:69]\n"
    "s_waitcnt lgkmcnt(12)\n"
    "v_mfma_f32_32x32x16_bf16 v[184:199], v[176:179], v[80:83], v[184:199]\n"
    "ds_read_b64_tr_b16 v[160:161], v251 offset:46080\n"
    "ds_read_b64_tr_b16 v[162:163], v253 offset:46080\n"
    "v_cndmask_b32_e64 v245, 0, v244, s[70:71]\n"
    "v_or_b32 v37, v37, v245\n"
    "s_waitcnt lgkmcnt(12)\n"
    "v_mfma_f32_32x32x16_bf16 v[184:199], v[180:183], v[84:87], v[184:199]\n"
    "ds_read_b64_tr_b16 v[164:165], v251 offset:48128\n"
    "ds_read_b64_tr_b16 v[166:167], v253 offset:48128\n"
    "v_cndmask_b32_e64 v38, 0, v243, s[72:73]\n"
    "v_cndmask_b32_e64 v245, 0, v244, s[74:75]\n"
    "s_waitcnt lgkmcnt(11)\n"
    "v_mfma_f32_32x32x16_bf16 v[184:199], v[144:147], v[88:91], v[184:199]\n"
    "ds_read_b64_tr_b16 v[168:169], v251 offset:50688\n"
    "ds_read_b64_tr_b16 v[170:171], v253 offset:50688\n"
    "v_or_b32 v38, v38, v245\n"
    "v_cndmask_b32_e64 v39, 0, v243, s[76:77]\n"
    "s_waitcnt lgkmcnt(10)\n"
    "v_mfma_f32_32x32x16_bf16 v[184:199], v[148:151], v[92:95], v[184:199]\n"
    "ds_read_b64_tr_b16 v[172:173], v251 offset:52736\n"
    "ds_read_b64_tr_b16 v[174:175], v253 offset:52736\n"
    "v_cndmask_b32_e64 v245, 0, v244, s[78:79]\n"
    "v_or_b32 v39, v39, v245\n"
    "s_waitcnt lgkmcnt(10)\n"
    "v_mfma_f32_32x32x16_bf16 v[184:199], v[152:155], v[96:99], v[184:199]\n"
    "ds_read_b64_tr_b16 v[176:177], v251 offset:54784\n"
    "ds_read_b64_tr_b16 v[178:179], v253 offset:54784\n"
    "v_cndmask_b32_e64 v40, 0, v243, s[80:81]\n"
    "v_cndmask_b32_e64 v245, 0, v244, s[82:83]\n"
    "s_waitcnt lgkmcnt(10)\n"
    "v_mfma_f32_32x32x16_bf16 v[184:199], v[156:159], v[100:103], v[184:199]\n"
    "ds_read_b64_tr_b16 v[180:181], v251 offset:56832\n"
    "ds_read_b64_tr_b16 v[182:183], v253 offset:56832\n"
    "v_or_b32 v40, v40, v245\n"
    "v_cndmask_b32_e64 v41, 0, v243, s[84:85]\n"
    "s_waitcnt lgkmcnt(10)\n"
    "v_mfma_f32_32x32x16_bf16 v[184:199], v[160:163], v[104:107], v[184:199]\n"
    "ds_read_b64_tr_b16 v[144:145], v251 offset:58880\n"
    "ds_read_b64_tr_b16 v[146:147], v253 offset:58880\n"
    "v_cndmask_b32_e64 v245, 0, v244, s[86:87]\n"
    "v_or_b32 v41, v41, v245\n"
    "s_waitcnt lgkmcnt(10)\n"
    "v_mfma_f32_32x32x16_bf16 v[184:199], v[164:167], v[108:111], v[184:199]\n"
    "ds_read_b64_tr_b16 v[148:149], v251 offset:60928\n"
    "ds_read_b64_tr_b16 v[150:151], v253 offset:60928\n"
    "v_cndmask_b32_e64 v42, 0, v243, s[88:89]\n"
    "v_cndmask_b32_e64 v245, 0, v244, s[90:91]\n"
    "s_waitcnt lgkmcnt(10)\n"
    "v_mfma_f32_32x32x16_bf16 v[184:199], v[168:171], v[112:115], v[184:199]\n"
    "ds_read_b64_tr_b16 v[152:153], v251 offset:62976\n"
    "ds_read_b64_tr_b16 v[154:155], v253 offset:62976\n"
    "v_or_b32 v42, v42, v245\n"
    "v_cndmask_b32_e64 v43, 0, v243, s[92:93]\n"
    "s_waitcnt lgkmcnt(10)\n"
    "v_mfma_f32_32x32x16_bf16 v[184:199], v[172:175], v[116:119], v[184:199]\n"
    "ds_read_b64_tr_b16 v[156:157], v251 offset:65024\n"
    "ds_read_b64_tr_b16 v[158:159], v253 offset:65024\n"
    "v_cndmask_b32_e64 v245, 0, v244, s[94:95]\n"
    "v_or_b32 v43, v43, v245\n"
    "s_waitcnt lgkmcnt(10)\n"
    "v_mfma_f32_32x32x16_bf16 v[184:199], v[176:179], v[120:123], v[184:199]\n"
    "v_mov_b32 v64, 0\n"
    "v_mov_b32 v65, s38\n"
    "v_mov_b32 v86, 0\n"
    "v_mov_b32 v87, s38\n"
    "s_waitcnt lgkmcnt(8)\n"
    "v_mfma_f32_32x32x16_bf16 v[184:199], v[180:183], v[124:127], v[184:199]\n"
    "s_and_b32 s55, s30, 1\n"
    "v_lshl_add_u32 v241, s55, 3, v233\n"
    "s_waitcnt lgkmcnt(6)\n"
    "v_mfma_f32_32x32x16_bf16 v[184:199], v[144:147], v[128:131], v[184:199]\n"
    "s_waitcnt lgkmcnt(4)\n"
    "v_mfma_f32_32x32x16_bf16 v[184:199], v[148:151], v[132:135], v[184:199]\n"
    "s_waitcnt lgkmcnt(2)\n"
    "v_mfma_f32_32x32x16_bf16 v[184:199], v[152:155], v[136:139], v[184:199]\n"
    "s_waitcnt lgkmcnt(0)\n"
    "v_mfma_f32_32x32x16_bf16 v[184:199], v[156:159], v[140:143], v[184:199]\n"
    "s_nop 11\n"
    "v_pk_mul_f32 v[184:185], v[184:185], s[98:99] op_sel_hi:[1,0]\n"
    "v_pk_mul_f32 v[186:187], v[186:187], s[98:99] op_sel_hi:[1,0]\n"
    "v_pk_mul_f32 v[188:189], v[188:189], s[98:99] op_sel_hi:[1,0]\n"
    "v_pk_mul_f32 v[190:191], v[190:191], s[98:99] op_sel_hi:[1,0]\n"
    "v_pk_mul_f32 v[192:193], v[192:193], s[98:99] op_sel_hi:[1,0]\n"
    "v_pk_mul_f32 v[194:195], v[194:195], s[98:99] op_sel_hi:[1,0]\n"
    "v_pk_mul_f32 v[196:197], v[196:197], s[98:99] op_sel_hi:[1,0]\n"
    "v_pk_mul_f32 v[198:199], v[198:199], s[98:99] op_sel_hi:[1,0]\n"
    "v_cvt_pk_bf16_f32 v184, v184, v185\n"
    "v_cvt_pk_bf16_f32 v185, v186, v187\n"
    "v_cvt_pk_bf16_f32 v186, v188, v189\n"
    "v_cvt_pk_bf16_f32 v187, v190, v191\n"
    "v_cvt_pk_bf16_f32 v188, v192, v193\n"
    "v_cvt_pk_bf16_f32 v189, v194, v195\n"
    "v_cvt_pk_bf16_f32 v190, v196, v197\n"
    "v_cvt_pk_bf16_f32 v191, v198, v199\n"
    "ds_write_b128 v252, v[184:187]\n"
    "ds_write_b128 v252, v[188:191] offset:1024\n"
    "s_waitcnt lgkmcnt(0)\n"
    "s_barrier\n"
    "ds_read_b128 v[66:69], v233 offset:3072\n"
    "ds_read_b128 v[74:77], v233 offset:4096\n"
    "ds_read_b128 v[78:81], v233 offset:5120\n"
    "ds_read_b128 v[82:85], v233 offset:6144\n"
    "s_mov_b32 exec_lo, 0xf000f\n"
    "s_mov_b32 exec_hi, 0xf000f\n"
    "ds_read_b64 v[86:87], v241 offset:7168\n"
    "s_mov_b64 exec, -1\n"
    "ds_read_b128 v[70:73], v235 offset:14336\n"
    "s_waitcnt lgkmcnt(0)\n"
    "v_add3_u32 v238, v66, v67, v68\n"
    "v_add3_u32 v238, v238, v69, v74\n"
    "v_add3_u32 v238, v238, v75, v76\n"
    "v_add3_u32 v238, v238, v77, v78\n"
    "v_add3_u32 v238, v238, v79, v80\n"
    "v_add3_u32 v238, v238, v81, v82\n"
    "v_add3_u32 v238, v238, v83, v84\n"
    "v_add_u32 v238, v238, v85\n"
    "v_sub_u32 v238, v238, v86\n"
    "s_nop 1\n"
    "v_add_u32_dpp v238, v238, v238 row_ror:8 row_mask:0xf bank_mask:0xf bound_ctrl:1\n"
    "s_nop 1\n"
    "v_add_u32_dpp v238, v238, v238 row_ror:4 row_mask:0xf bank_mask:0xf bound_ctrl:1\n"
    "s_nop 1\n"
    "v_add_u32_dpp v238, v238, v238 row_ror:2 row_mask:0xf bank_mask:0xf bound_ctrl:1\n"
    "s_nop 1\n"
    "v_add_u32_dpp v238, v238, v238 row_ror:1 row_mask:0xf bank_mask:0xf bound_ctrl:1\n"
    "v_bitop3_b32 v238, v238, v87, s38 bitop3:0xf6\n"
    "v_cmp_ne_u32 vcc, 0, v238\n"
    "s_cbranch_vccnz .Lkr15\n"
    ".Lkb16:\n"
    "s_cmp_lg_u32 s51, 0\n"
    "s_cbranch_scc1 .Lkf17\n"
    ".Lkfb18:\n"
    "s_sleep 4\n"
    "s_add_u32 m0, s1, 2048\n"
    "s_sub_u32 s61, s3, 460544\n"
    "s_max_i32 s61, s61, s4\n"
    "s_add_u32 s62, s16, s61\n"
    "s_addc_u32 s63, s17, 0\n"
    "global_load_lds_dwordx4 v2, s[62:63] offset:1024 sc1\n"
    "s_add_u32 m0, s1, 3072\n"
    "s_sub_u32 s61, s3, 197376\n"
    "s_max_i32 s61, s61, s4\n"
    "s_add_u32 s62, s16, s61\n"
    "s_addc_u32 s63, s17, 0\n"
    "global_load_lds_dwordx4 v2, s[62:63] offset:1024 sc1\n"
    "s_add_u32 m0, s1, 4096\n"
    "s_sub_u32 s61, s3, 131584\n"
    "s_max_i32 s61, s61, s4\n"
    "s_add_u32 s62, s16, s61\n"
    "s_addc_u32 s63, s17, 0\n"
    "global_load_lds_dwordx4 v2, s[62:63] offset:1024 sc1\n"
    "s_add_u32 m0, s1, 5120\n"
    "s_sub_u32 s61, s3, 65792\n"
    "s_max_i32 s61, s61, s4\n"
    "s_add_u32 s62, s16, s61\n"
    "s_addc_u32 s63, s17, 0\n"
    "global_load_lds_dwordx4 v2, s[62:63] offset:1024 sc1\n"
    "s_mov_b32 exec_lo, 0xf000f\n"
    "s_mov_b32 exec_hi, 0xf000f\n"
    "global_load_dwordx2 v[64:65], v15, s[16:17] offset:128 sc1\n"
    "s_mov_b64 exec, -1\n"
    "s_branch .Latt\n"
    ".LattH:\n"
    "s_cmp_eq_u32 s30, 0\n"
    "s_cselect_b32 s56, s99, 0\n"
    "s_cmp_eq_u32 s56, 0\n"
    "s_waitcnt lgkmcnt(0)\n"
    "s_barrier\n"
    "ds_read_b128 v[66:69], v235 offset:14336\n"
    "s_cbranch_scc1 .Latt\n"
    "ds_read_b32 v106, v165\n"
    "ds_read_b32 v107, v165 offset:256\n"
    "ds_read_b32 v108, v165 offset:512\n"
    "ds_read_b32 v109, v165 offset:768\n"
    "v_mov_b32 v111, s38\n"
    "s_waitcnt lgkmcnt(0)\n"
    "v_add_f32 v106, v106, v107\n"
    "v_add_f32 v108, v108, v109\n"
    "v_add_f32 v110, v106, v108\n"
    "global_store_dwordx2 v164, v[110:111], s[16:17] sc1\n"
    ".Latt:\n"
    "ds_read_b128 v[190:193], v235\n"
    "ds_read_b128 v[194:197], v236\n"
    "s_waitcnt lgkmcnt(0)\n"
    "v_mfma_f32_32x32x16_bf16 v[118:133], v[66:69], v[194:197], 0\n"
    "v_mfma_f32_32x32x16_bf16 v[86:101], v[66:69], v[190:193], 0\n"
    "v_mfma_f32_32x32x16_bf16 v[102:117], v[70:73], v[190:193], 0\n"
    "v_mfma_f32_32x32x16_bf16 v[134:149], v[74:77], v[194:197], 0\n"
    "s_setprio 0\n"
    "s_nop 7\n"
    "v_exp_f32 v118, v118\n"
    "v_exp_f32 v119, v119\n"
    "v_exp_f32 v120, v120\n"
    "v_cvt_pk_bf16_f32 v118, v118, v119\n"
    "v_exp_f32 v121, v121\n"
    "v_exp_f32 v122, v122\n"
    "v_cvt_pk_bf16_f32 v119, v120, v121\n"
    "v_exp_f32 v123, v123\n"
    "v_exp_f32 v124, v124\n"
    "v_cvt_pk_bf16_f32 v120, v122, v123\n"
    "v_exp_f32 v125, v125\n"
    "v_exp_f32 v126, v126\n"
    "v_cvt_pk_bf16_f32 v121, v124, v125\n"
    "v_exp_f32 v127, v127\n"
    "v_exp_f32 v128, v128\n"
    "v_cvt_pk_bf16_f32 v122, v126, v127\n"
    "v_exp_f32 v129, v129\n"
    "v_exp_f32 v130, v130\n"
    "v_cvt_pk_bf16_f32 v123, v128, v129\n"
    "v_exp_f32 v131, v131\n"
    "v_exp_f32 v132, v132\n"
    "v_cvt_pk_bf16_f32 v124, v130, v131\n"
    "v_exp_f32 v133, v133\n"
    "s_nop 0\n"
    "v_cvt_pk_bf16_f32 v125, v132, v133\n"
    "v_cndmask_b32_e64 v86, v102, v86, s[64:65]\n"
    "v_cndmask_b32_e64 v87, v103, v87, s[66:67]\n"
    "v_cndmask_b32_e64 v88, v104, v88, s[68:69]\n"
    "v_cndmask_b32_e64 v89, v105, v89, s[70:71]\n"
    "v_cndmask_b32_e64 v90, v106, v90, s[72:73]\n"
    "v_cndmask_b32_e64 v91, v107, v91, s[74:75]\n"
    "v_cndmask_b32_e64 v92, v108, v92, s[76:77]\n"
    "v_cndmask_b32_e64 v93, v109, v93, s[78:79]\n"
    "v_cndmask_b32_e64 v94, v110, v94, s[80:81]\n"
    "v_cndmask_b32_e64 v95, v111, v95, s[82:83]\n"
    "v_cndmask_b32_e64 v96, v112, v96, s[84:85]\n"
    "v_cndmask_b32_e64 v97, v113, v97, s[86:87]\n"
    "v_cndmask_b32_e64 v98, v114, v98, s[88:89]\n"
    "v_cndmask_b32_e64 v99, v115, v99, s[90:91]\n"
    "v_cndmask_b32_e64 v100, v116, v100, s[92:93]\n"
    "v_cndmask_b32_e64 v101, v117, v101, s[94:95]\n"
    "v_mfma_f32_32x32x16_bf16 v[102:117], v[78:81], v[194:197], 0\n"
    "v_exp_f32 v86, v86\n"
    "v_exp_f32 v87, v87\n"
    "v_exp_f32 v88, v88\n"
    "v_cvt_pk_bf16_f32 v86, v86, v87\n"
    "v_exp_f32 v89, v89\n"
    "v_exp_f32 v90, v90\n"
    "v_cvt_pk_bf16_f32 v87, v88, v89\n"
    "v_exp_f32 v91, v91\n"
    "v_exp_f32 v92, v92\n"
    "v_cvt_pk_bf16_f32 v88, v90, v91\n"
    "v_exp_f32 v93, v93\n"
    "v_exp_f32 v94, v94\n"
    "v_cvt_pk_bf16_f32 v89, v92, v93\n"
    "v_exp_f32 v95, v95\n"
    "v_exp_f32 v96, v96\n"
    "v_cvt_pk_bf16_f32 v90, v94, v95\n"
    "v_exp_f32 v97, v97\n"
    "v_exp_f32 v98, v98\n"
    "v_cvt_pk_bf16_f32 v91, v96, v97\n"
    "v_exp_f32 v99, v99\n"
    "v_exp_f32 v100, v100\n"
    "v_cvt_pk_bf16_f32 v92, v98, v99\n"
    "v_exp_f32 v101, v101\n"
    "s_nop 0\n"
    "v_cvt_pk_bf16_f32 v93, v100, v101\n"
    "s_cmp_ge_u32 s30, 4\n"
    "s_cbranch_scc1 .Lhv25\n"
    "s_add_u32 m0, s1, 2048\n"
    "s_sub_u32 s61, s3, 526336\n"
    "s_max_i32 s61, s61, s4\n"
    "s_add_u32 s62, s16, s61\n"
    "s_addc_u32 s63, s17, 0\n"
    "global_load_lds_dwordx4 v2, s[62:63] offset:1024 sc1\n"
    "s_add_u32 m0, s1, 3072\n"
    "s_sub_u32 s61, s3, 394752\n"
    "s_max_i32 s61, s61, s4\n"
    "s_add_u32 s62, s16, s61\n"
    "s_addc_u32 s63, s17, 0\n"
    "global_load_lds_dwordx4 v2, s[62:63] offset:1024 sc1\n"
    "s_add_u32 m0, s1, 4096\n"
    "s_sub_u32 s61, s3, 328960\n"
    "s_max_i32 s61, s61, s4\n"
    "s_add_u32 s62, s16, s61\n"
    "s_addc_u32 s63, s17, 0\n"
    "global_load_lds_dwordx4 v2, s[62:63] offset:1024 sc1\n"
    "s_add_u32 m0, s1, 5120\n"
    "s_sub_u32 s61, s3, 263168\n"
    "s_max_i32 s61, s61, s4\n"
    "s_add_u32 s62, s16, s61\n"
    "s_addc_u32 s63, s17, 0\n"
    "global_load_lds_dwordx4 v2, s[62:63] offset:1024 sc1\n"
    "s_mov_b32 exec_lo, 0xf000f\n"
    "s_mov_b32 exec_hi, 0xf000f\n"
    "global_load_dwordx2 v[64:65], v15, s[16:17] offset:128 sc1\n"
    "s_mov_b64 exec, -1\n"
    ".Lhv25:\n"
    "v_and_b32 v182, v36, v86\n"
    "v_and_b32 v183, v37, v87\n"
    "v_and_b32 v184, v38, v88\n"
    "v_and_b32 v185, v39, v89\n"
    "v_and_b32 v186, v40, v90\n"
    "v_and_b32 v187, v41, v91\n"
    "v_and_b32 v188, v42, v92\n"
    "v_and_b32 v189, v43, v93\n"
    "v_xor_b32 v86, v182, v86\n"
    "v_xor_b32 v87, v183, v87\n"
    "v_xor_b32 v88, v184, v88\n"
    "v_xor_b32 v89, v185, v89\n"
    "v_xor_b32 v90, v186, v90\n"
    "v_xor_b32 v91, v187, v91\n"
    "v_xor_b32 v92, v188, v92\n"
    "v_xor_b32 v93, v189, v93\n"
    "v_exp_f32 v134, v134\n"
    "v_exp_f32 v135, v135\n"
    "v_exp_f32 v136, v136\n"
    "v_cvt_pk_bf16_f32 v134, v134, v135\n"
    "v_exp_f32 v137, v137\n"
    "s_or_b32 m0, s7, 65536\n"
    "s_lshl3_add_u32 s62, s7, s18\n"
    "s_addc_u32 s63, s19, 0\n"
    "global_load_lds_dwordx4 v11, s[62:63]\n"
    "s_add_u32 s7, s7, 8192\n"
    "s_and_b32 s7, s7, 0xffff\n"
    "v_exp_f32 v138, v138\n"
    "v_cvt_pk_bf16_f32 v135, v136, v137\n"
    "v_exp_f32 v139, v139\n"
    "v_exp_f32 v140, v140\n"
    "v_cvt_pk_bf16_f32 v136, v138, v139\n"
    "v_exp_f32 v141, v141\n"
    "v_exp_f32 v142, v142\n"
    "s_or_b32 m0, s7, 65536\n"
    "s_lshl3_add_u32 s62, s7, s18\n"
    "s_addc_u32 s63, s19, 0\n"
    "global_load_lds_dwordx4 v11, s[62:63]\n"
    "s_add_u32 s7, s7, 8192\n"
    "s_and_b32 s7, s7, 0xffff\n"
    "v_cvt_pk_bf16_f32 v137, v140, v141\n"
    "v_exp_f32 v143, v143\n"
    "v_exp_f32 v144, v144\n"
    "v_cvt_pk_bf16_f32 v138, v142, v143\n"
    "v_exp_f32 v145, v145\n"
    "v_exp_f32 v146, v146\n"
    "v_cvt_pk_bf16_f32 v139, v144, v145\n"
    "v_exp_f32 v147, v147\n"
    "v_exp_f32 v148, v148\n"
    "v_cvt_pk_bf16_f32 v140, v146, v147\n"
    "v_exp_f32 v149, v149\n"
    "s_nop 0\n"
    "v_cvt_pk_bf16_f32 v141, v148, v149\n"
    "s_waitcnt vmcnt(2)\n"
    ".Lvb27:\n"
    "ds_read_b128 v[150:153], v233 offset:3072\n"
    "ds_read_b128 v[154:157], v233 offset:4096\n"
    "ds_read_b128 v[158:161], v233 offset:5120\n"
    "ds_read_b128 v[162:165], v233 offset:6144\n"
    "ds_read_b128 v[44:47], v249 offset:0\n"
    "ds_read_b128 v[16:19], v249 offset:256\n"
    "ds_read_b128 v[48:51], v250 offset:0\n"
    "ds_read_b128 v[20:23], v250 offset:256\n"
    "ds_read_b128 v[52:55], v237 offset:1024\n"
    "ds_read_b128 v[24:27], v237 offset:1280\n"
    "ds_read_b128 v[56:59], v237 offset:2048\n"
    "ds_read_b128 v[28:31], v237 offset:2304\n"
    "ds_read_b128 v[60:63], v237 offset:3072\n"
    "ds_read_b128 v[32:35], v237 offset:3328\n"
    "s_waitcnt lgkmcnt(10)\n"
    "v_add3_u32 v238, v150, v151, v152\n"
    "v_add3_u32 v238, v238, v153, v154\n"
    "v_add3_u32 v238, v238, v155, v156\n"
    "v_add3_u32 v238, v238, v157, v158\n"
    "v_add3_u32 v238, v238, v159, v160\n"
    "v_add3_u32 v238, v238, v161, v162\n"
    "v_add3_u32 v238, v238, v163, v164\n"
    "v_add_u32 v238, v238, v165\n"
    "v_sub_u32 v238, v238, v64\n"
    "s_nop 1\n"
    "v_add_u32_dpp v238, v238, v238 row_ror:8 row_mask:0xf bank_mask:0xf bound_ctrl:1\n"
    "s_nop 1\n"
    "v_add_u32_dpp v238, v238, v238 row_ror:4 row_mask:0xf bank_mask:0xf bound_ctrl:1\n"
    "s_nop 1\n"
    "v_add_u32_dpp v238, v238, v238 row_ror:2 row_mask:0xf bank_mask:0xf bound_ctrl:1\n"
    "s_nop 1\n"
    "v_add_u32_dpp v238, v238, v238 row_ror:1 row_mask:0xf bank_mask:0xf bound_ctrl:1\n"
    "v_bitop3_b32 v238, v238, v65, s38 bitop3:0xf6\n"
    "v_cmp_ne_u32 vcc, 0, v238\n"
    "s_waitcnt lgkmcnt(0)\n"
    "s_and_b64 vcc, vcc, s[52:53]\n"
    "s_cbranch_scc1 .Lvr26\n"
    "s_cmp_lg_u32 s51, 0\n"
    "s_cbranch_scc1 .Lvf28\n"
    ".Lvfb29:\n"
    "v_mfma_f32_32x32x16_bf16 v[166:181], v[44:47], v[118:121], 0\n"
    "v_exp_f32 v102, v102\n"
    "v_exp_f32 v103, v103\n"
    "v_exp_f32 v104, v104\n"
    "v_mfma_f32_32x32x16_bf16 v[166:181], v[16:19], v[122:125], v[166:181]\n"
    "v_cvt_pk_bf16_f32 v102, v102, v103\n"
    "v_exp_f32 v105, v105\n"
    "v_exp_f32 v106, v106\n"
    "v_cvt_pk_bf16_f32 v103, v104, v105\n"
    "v_exp_f32 v107, v107\n"
    "v_mfma_f32_32x32x16_bf16 v[118:133], v[82:85], v[194:197], 0\n"
    "v_exp_f32 v108, v108\n"
    "s_or_b32 m0, s7, 65536\n"
    "s_lshl3_add_u32 s62, s7, s18\n"
    "s_addc_u32 s63, s19, 0\n"
    "global_load_lds_dwordx4 v11, s[62:63]\n"
    "s_add_u32 s7, s7, 8192\n"
    "s_and_b32 s7, s7, 0xffff\n"
    "v_cvt_pk_bf16_f32 v104, v106, v107\n"
    "v_exp_f32 v109, v109\n"
    "v_exp_f32 v110, v110\n"
    "v_mfma_f32_32x32x16_bf16 v[150:165], v[48:51], v[86:89], 0\n"
    "v_cvt_pk_bf16_f32 v105, v108, v109\n"
    "v_exp_f32 v111, v111\n"
    "v_exp_f32 v112, v112\n"
    "v_cvt_pk_bf16_f32 v106, v110, v111\n"
    "v_exp_f32 v113, v113\n"
    "v_mfma_f32_32x32x16_bf16 v[150:165], v[20:23], v[90:93], v[150:165]\n"
    "v_exp_f32 v114, v114\n"
    "s_or_b32 m0, s7, 65536\n"
    "s_lshl3_add_u32 s62, s7, s18\n"
    "s_addc_u32 s63, s19, 0\n"
    "global_load_lds_dwordx4 v11, s[62:63]\n"
    "s_add_u32 s7, s7, 8192\n"
    "s_and_b32 s7, s7, 0xffff\n"
    "v_cvt_pk_bf16_f32 v107, v112, v113\n"
    "v_exp_f32 v115, v115\n"
    "v_exp_f32 v116, v116\n"
    "v_mfma_f32_32x32x16_bf16 v[86:101], v[74:77], v[190:193], 0\n"
    "v_cvt_pk_bf16_f32 v108, v114, v115\n"
    "v_exp_f32 v117, v117\n"
    "global_load_dwordx4 v[208:211], v13, s[20:21]\n"
    "v_cvt_pk_bf16_f32 v109, v116, v117\n"
    "v_exp_f32 v118, v118\n"
    "v_exp_f32 v119, v119\n"
    "v_mfma_f32_32x32x16_bf16 v[166:181], v[52:55], v[134:137], v[166:181]\n"
    "v_exp_f32 v120, v120\n"
    "s_or_b32 m0, s7, 65536\n"
    "s_lshl3_add_u32 s62, s7, s18\n"
    "s_addc_u32 s63, s19, 0\n"
    "global_load_lds_dwordx4 v11, s[62:63]\n"
    "s_add_u32 s7, s7, 8192\n"
    "s_and_b32 s7, s7, 0xffff\n"
    "v_cvt_pk_bf16_f32 v118, v118, v119\n"
    "v_exp_f32 v121, v121\n"
    "v_exp_f32 v122, v122\n"
    "v_mfma_f32_32x32x16_bf16 v[166:181], v[24:27], v[138:141], v[166:181]\n"
    "v_cvt_pk_bf16_f32 v119, v120, v121\n"
    "v_exp_f32 v123, v123\n"
    "s_or_b32 m0, s7, 65536\n"
    "s_lshl3_add_u32 s62, s7, s18\n"
    "s_addc_u32 s63, s19, 0\n"
    "global_load_lds_dwordx4 v11, s[62:63]\n"
    "s_add_u32 s7, s7, 8192\n"
    "s_and_b32 s7, s7, 0xffff\n"
    "v_exp_f32 v124, v124\n"
    "v_cvt_pk_bf16_f32 v120, v122, v123\n"
    "v_exp_f32 v125, v125\n"
    "v_mfma_f32_32x32x16_bf16 v[134:149], v[78:81], v[190:193], 0\n"
    "v_exp_f32 v126, v126\n"
    "s_or_b32 m0, s7, 65536\n"
    "s_lshl3_add_u32 s62, s7, s18\n"
    "s_addc_u32 s63, s19, 0\n"
    "global_load_lds_dwordx4 v11, s[62:63]\n"
    "s_add_u32 s7, s7, 8192\n"
    "s_and_b32 s7, s7, 0xffff\n"
    "v_cvt_pk_bf16_f32 v121, v124, v125\n"
    "v_exp_f32 v127, v127\n"
    "v_exp_f32 v128, v128\n"
    "v_mfma_f32_32x32x16_bf16 v[166:181], v[56:59], v[102:105], v[166:181]\n"
    "v_cvt_pk_bf16_f32 v122, v126, v127\n"
    "v_exp_f32 v129, v129\n"
    "s_or_b32 m0, s7, 65536\n"
    "s_lshl3_add_u32 s62, s7, s18\n"
    "s_addc_u32 s63, s19, 0\n"
    "global_load_lds_dwordx4 v11, s[62:63]\n"
    "s_add_u32 s7, s7, 8192\n"
    "s_and_b32 s7, s7, 0xffff\n"
    "v_exp_f32 v130, v130\n"
    "v_cvt_pk_bf16_f32 v123, v128, v129\n"
    "v_exp_f32 v131, v131\n"
    "v_mfma_f32_32x32x16_bf16 v[166:181], v[28:31], v[106:109], v[166:181]\n"
    "v_exp_f32 v132, v132\n"
    "global_load_dwordx4 v[204:207], v12, s[8:9]\n"
    "v_cvt_pk_bf16_f32 v124, v130, v131\n"
    "v_exp_f32 v133, v133\n"
    "s_nop 0\n"
    "v_cvt_pk_bf16_f32 v125, v132, v133\n"
    "v_exp_f32 v86, v86\n"
    "v_mfma_f32_32x32x16_bf16 v[166:181], v[60:63], v[118:121], v[166:181]\n"
    "v_exp_f32 v87, v87\n"
    "s_add_u32 s62, s16, s15\n"
    "s_addc_u32 s63, s17, 0\n"
    "global_load_dwordx4 v[212:215], v14, s[62:63] sc1\n"
    "v_exp_f32 v88, v88\n"
    "v_cvt_pk_bf16_f32 v86, v86, v87\n"
    "v_exp_f32 v89, v89\n"
    "v_mfma_f32_32x32x16_bf16 v[166:181], v[32:35], v[122:125], v[166:181]\n"
    "v_exp_f32 v90, v90\n"
    "v_cvt_pk_bf16_f32 v87, v88, v89\n"
    "v_exp_f32 v91, v91\n"
    "v_exp_f32 v92, v92\n"
    "v_mfma_f32_32x32x16_bf16 v[102:117], v[82:85], v[190:193], 0\n"
    "v_cvt_pk_bf16_f32 v88, v90, v91\n"
    "v_exp_f32 v93, v93\n"
    "v_exp_f32 v94, v94\n"
    "v_cvt_pk_bf16_f32 v89, v92, v93\n"
    "v_exp_f32 v95, v95\n"
    "v_mfma_f32_32x32x16_bf16 v[150:165], v[44:47], v[182:185], v[150:165]\n"
    "v_exp_f32 v96, v96\n"
    "v_cvt_pk_bf16_f32 v90, v94, v95\n"
    "v_exp_f32 v97, v97\n"
    "v_exp_f32 v98, v98\n"
    "v_mfma_f32_32x32x16_bf16 v[150:165], v[16:19], v[186:189], v[150:165]\n"
    "v_cvt_pk_bf16_f32 v91, v96, v97\n"
    "v_exp_f32 v99, v99\n"
    "ds_write_b128 v233, v[166:169]\n"
    "v_exp_f32 v100, v100\n"
    "ds_write_b128 v233, v[170:173] offset:1024\n"
    "v_cvt_pk_bf16_f32 v92, v98, v99\n"
    "v_exp_f32 v101, v101\n"
    "ds_write_b32 v233, v174 offset:2048\n"
    "v_cvt_pk_bf16_f32 v93, v100, v101\n"
    "v_exp_f32 v134, v134\n"
    "v_exp_f32 v135, v135\n"
    "v_mfma_f32_32x32x16_bf16 v[150:165], v[52:55], v[86:89], v[150:165]\n"
    "v_exp_f32 v136, v136\n"
    "v_cvt_pk_bf16_f32 v134, v134, v135\n"
    "v_exp_f32 v137, v137\n"
    "v_exp_f32 v138, v138\n"
    "v_mfma_f32_32x32x16_bf16 v[150:165], v[24:27], v[90:93], v[150:165]\n"
    "v_cvt_pk_bf16_f32 v135, v136, v137\n"
    "v_exp_f32 v139, v139\n"
    "v_exp_f32 v140, v140\n"
    "s_cmp_eq_u32 s30, 0\n"
    "s_cbranch_scc1 .Lh3\n"
    "s_waitcnt vmcnt(0) lgkmcnt(0)\n"
    "s_barrier\n"
    ".Lh3b:\n"
    "ds_read_b32 v174, v234 offset:2048\n"
    "ds_read_b128 v[166:169], v234\n"
    "ds_read_b128 v[170:173], v234 offset:1024\n"
    "v_cvt_pk_bf16_f32 v136, v138, v139\n"
    "v_exp_f32 v141, v141\n"
    "v_exp_f32 v142, v142\n"
    "s_mov_b64 exec, 0xffff\n"
    "global_load_dword v4, v229, s[40:41]\n"
    "s_mov_b64 exec, -1\n"
    "v_cvt_pk_bf16_f32 v137, v140, v141\n"
    "v_exp_f32 v143, v143\n"
    "v_exp_f32 v144, v144\n"
    "v_cvt_pk_bf16_f32 v138, v142, v143\n"
    "v_exp_f32 v145, v145\n"
    "v_exp_f32 v146, v146\n"
    "v_cvt_pk_bf16_f32 v139, v144, v145\n"
    "v_exp_f32 v147, v147\n"
    "v_exp_f32 v148, v148\n"
    "v_cvt_pk_bf16_f32 v140, v146, v147\n"
    "v_exp_f32 v149, v149\n"
    "s_nop 0\n"
    "v_cvt_pk_bf16_f32 v141, v148, v149\n"
    "v_exp_f32 v102, v102\n"
    "v_exp_f32 v103, v103\n"
    "v_mfma_f32_32x32x16_bf16 v[150:165], v[56:59], v[134:137], v[150:165]\n"
    "v_exp_f32 v104, v104\n"
    "v_cvt_pk_bf16_f32 v102, v102, v103\n"
    "v_exp_f32 v105, v105\n"
    "v_exp_f32 v106, v106\n"
    "v_mfma_f32_32x32x16_bf16 v[150:165], v[28:31], v[138:141], v[150:165]\n"
    "v_cvt_pk_bf16_f32 v103, v104, v105\n"
    "v_exp_f32 v107, v107\n"
    "v_exp_f32 v108, v108\n"
    "v_cvt_pk_bf16_f32 v104, v106, v107\n"
    "v_exp_f32 v109, v109\n"
    "v_exp_f32 v110, v110\n"
    "v_cvt_pk_bf16_f32 v105, v108, v109\n"
    "v_exp_f32 v111, v111\n"
    "v_exp_f32 v112, v112\n"
    "v_mfma_f32_32x32x16_bf16 v[150:165], v[60:63], v[102:105], v[150:165]\n"
    "v_cvt_pk_bf16_f32 v106, v110, v111\n"
    "v_exp_f32 v113, v113\n"
    "v_exp_f32 v114, v114\n"
    "v_cvt_pk_bf16_f32 v107, v112, v113\n"
    "v_exp_f32 v115, v115\n"
    "v_exp_f32 v116, v116\n"
    "v_cvt_pk_bf16_f32 v108, v114, v115\n"
    "v_exp_f32 v117, v117\n"
    "v_mov_b32 v241, s38\n"
    "v_cvt_pk_bf16_f32 v109, v116, v117\n"
    "s_nop 1\n"
    "v_mfma_f32_32x32x16_bf16 v[150:165], v[32:35], v[106:109], v[150:165]\n"
    "s_add_u32 s62, s16, s13\n"
    "s_addc_u32 s63, s17, 0\n"
    "s_waitcnt lgkmcnt(0)\n"
    "s_nop 8\n"
    "v_add_f32 v174, v174, v158\n"
    "v_subrev_u32 v238, 562036736, v174\n"
    "v_cmp_lt_u32 vcc, 838860800, v238\n"
    "s_cbranch_vccnz .Lex37\n"
    ".Lexb38:\n"
    "v_rcp_f32 v174, v174\n"
    "v_add_f32 v166, v166, v150\n"
    "v_add_f32 v167, v167, v151\n"
    "v_add_f32 v168, v168, v152\n"
    "v_add_f32 v169, v169, v153\n"
    "v_add_f32 v170, v170, v154\n"
    "v_add_f32 v171, v171, v155\n"
    "v_add_f32 v172, v172, v156\n"
    "v_add_f32 v173, v173, v157\n"
    "v_mul_f32 v166, v174, v166\n"
    "v_mul_f32 v167, v174, v167\n"
    "v_mul_f32 v168, v174, v168\n"
    "v_mul_f32 v169, v174, v169\n"
    "v_mul_f32 v170, v174, v170\n"
    "v_mul_f32 v171, v174, v171\n"
    "v_mul_f32 v172, v174, v172\n"
    "v_mul_f32 v173, v174, v173\n"
    "v_cvt_pk_bf16_f32 v166, v166, v167\n"
    "v_cvt_pk_bf16_f32 v167, v168, v169\n"
    "v_cvt_pk_bf16_f32 v168, v170, v171\n"
    "v_cvt_pk_bf16_f32 v169, v172, v173\n"
    "global_store_dwordx4 v2, v[166:169], s[62:63] sc1\n"
    "s_add_u32 s62, s16, s14\n"
    "s_addc_u32 s63, s17, 0\n"
    "s_mov_b32 vcc_lo, 0xcccccccc\n"
    "s_mov_b32 vcc_hi, 0xcccccccc\n"
    "v_add3_u32 v240, v166, v167, v168\n"
    "v_add_u32 v240, v240, v169\n"
    "s_nop 1\n"
    "v_add_u32_dpp v240, v240, v240 row_ror:8 row_mask:0xf bank_mask:0xf bound_ctrl:1\n"
    "s_nop 1\n"
    "v_add_u32_dpp v240, v240, v240 row_ror:4 row_mask:0xf bank_mask:0xf bound_ctrl:1\n"
    "s_nop 1\n"
    "v_add_u32_dpp v240, v240, v240 row_ror:2 row_mask:0xf bank_mask:0xf bound_ctrl:1\n"
    "s_nop 1\n"
    "v_add_u32_dpp v240, v240, v240 row_ror:1 row_mask:0xf bank_mask:0xf bound_ctrl:1\n"
    "v_cndmask_b32 v240, v240, v241, vcc\n"
    "v_lshrrev_b32 v240, v2, v240\n"
    "global_store_short v10, v240, s[62:63] sc1\n"
    "v_xor_b32 v190, 64, v3\n"
    "ds_read_b64_tr_b16 v[16:17], v3 offset:0\n"
    "ds_read_b64_tr_b16 v[18:19], v3 offset:1024\n"
    "ds_read_b64_tr_b16 v[24:25], v3 offset:2048\n"
    "ds_read_b64_tr_b16 v[26:27], v3 offset:3072\n"
    "ds_read_b64_tr_b16 v[32:33], v3 offset:4096\n"
    "ds_read_b64_tr_b16 v[34:35], v3 offset:5120\n"
    "ds_read_b64_tr_b16 v[40:41], v3 offset:6144\n"
    "ds_read_b64_tr_b16 v[42:43], v3 offset:7168\n"
    "ds_read_b64_tr_b16 v[48:49], v3 offset:8192\n"
    "ds_read_b64_tr_b16 v[50:51], v3 offset:9216\n"
    "ds_read_b64_tr_b16 v[56:57], v3 offset:10240\n"
    "ds_read_b64_tr_b16 v[58:59], v3 offset:11264\n"
    "v_xor_b32 v238, s38, v213\n"
    "v_bitop3_b32 v238, v238, v215, s38 bitop3:0xf6\n"
    "v_cmp_ne_u32 vcc, 0, v238\n"
    "s_cbranch_vccnz .Lgr44\n"
    ".Lgb45:\n"
    "ds_read_b64_tr_b16 v[64:65], v3 offset:12288\n"
    "v_mov_b32 v213, v212\n"
    "v_mov_b32 v215, v214\n"
    "s_nop 0\n"
    "v_permlane16_swap_b32 v212, v213\n"
    "ds_read_b64_tr_b16 v[66:67], v3 offset:13312\n"
    "v_permlane16_swap_b32 v214, v215\n"
    "v_add_f32 v212, v212, v213\n"
    "v_add_f32 v214, v214, v215\n"
    "ds_read_b64_tr_b16 v[72:73], v3 offset:14336\n"
    "v_mov_b32 v213, v212\n"
    "v_mov_b32 v215, v214\n"
    "s_nop 0\n"
    "v_permlane32_swap_b32 v212, v213\n"
    "ds_read_b64_tr_b16 v[74:75], v3 offset:15360\n"
    "v_permlane32_swap_b32 v214, v215\n"
    "v_add_f32 v212, v212, v213\n"
    "v_add_f32 v214, v214, v215\n"
    "ds_read_b64_tr_b16 v[20:21], v190 offset:0\n"
    "v_cvt_pk_bf16_f32 v213, v212, v214\n"
    "s_lshr_b32 s54, s30, 1\n"
    "s_cmp_eq_u32 s54, 2\n"
    "ds_read_b64_tr_b16 v[22:23], v190 offset:1024\n"
    "s_cselect_b32 s54, 1.0, 0\n"
    "v_lshlrev_b32 v212, 16, v213\n"
    "v_and_b32 v213, s50, v213\n"
    "ds_read_b64_tr_b16 v[28:29], v190 offset:2048\n"
    "v_add_f32 v212, s54, v212\n"
    "v_add_f32 v213, s54, v213\n"
    "v_cvt_pk_bf16_f32 v213, v212, v213\n"
    "ds_read_b64_tr_b16 v[30:31], v190 offset:3072\n"
    "s_lshl_b32 s54, s30, 7\n"
    "s_add_u32 s54, s54, 145408\n"
    "v_and_b32 v238, 15, v1\n"
    "ds_read_b64_tr_b16 v[36:37], v190 offset:4096\n"
    "v_lshl_add_u32 v238, v238, 3, s54\n"
    "v_lshlrev_b32 v212, 16, v213\n"
    "v_and_b32 v213, s50, v213\n"
    "ds_read_b64_tr_b16 v[38:39], v190 offset:5120\n"
    "ds_write_b64 v238, v[212:213]\n"
    "v_and_b32 v239, 7, v1\n"
    "s_lshl1_add_u32 s54, s36, s44\n"
    "ds_read_b64_tr_b16 v[44:45], v190 offset:6144\n"
    "s_mul_i32 s55, s54, 33024\n"
    "s_lshl_b32 s56, s45, 13\n"
    "s_add_u32 s55, s55, s56\n"
    "ds_read_b64_tr_b16 v[46:47], v190 offset:7168\n"
    "s_add_u32 s55, s55, 7344128\n"
    "v_add_u32 v191, s55, v2\n"
    "s_lshl2_add_u32 s54, s54, s45\n"
    "ds_read_b64_tr_b16 v[52:53], v190 offset:8192\n"
    "s_lshl_b32 s54, s54, 10\n"
    "s_add_u32 s54, s54, 589824\n"
    "v_lshl_add_u32 v192, v239, 7, s54\n"
    "ds_read_b64_tr_b16 v[54:55], v190 offset:9216\n"
    "v_lshrrev_b32 v240, 4, v1\n"
    "v_lshl_add_u32 v192, v240, 5, v192\n"
    "v_lshlrev_b32 v193, 5, v239\n"
    "ds_read_b64_tr_b16 v[60:61], v190 offset:10240\n"
    "v_add_u32 v193, 145408, v193\n"
    "v_mov_b32 v144, 0\n"
    "v_mov_b32 v145, s38\n"
    "ds_read_b64_tr_b16 v[62:63], v190 offset:11264\n"
    "v_lshlrev_b32 v180, 16, v208\n"
    "s_cmp_lg_u32 s30, 0\n"
    "s_cbranch_scc1 .Lh3c\n"
    "s_lshl_b32 s58, s36, 10\n"
    "s_add_u32 s58, s58, 1114112\n"
    "s_add_u32 s62, s16, s58\n"
    "s_addc_u32 s63, s17, 0\n"
    "s_load_dword s80, s[62:63], 0x0\n"
    "s_load_dword s81, s[62:63], 0x80\n"
    "s_load_dword s82, s[62:63], 0x100\n"
    "s_load_dword s83, s[62:63], 0x180\n"
    "s_load_dword s84, s[62:63], 0x200\n"
    "s_load_dword s85, s[62:63], 0x280\n"
    "s_load_dword s86, s[62:63], 0x300\n"
    "s_load_dword s87, s[62:63], 0x380\n"
    ".Lh3c:\n"
    "v_and_b32 v181, s50, v208\n"
    "ds_read_b64_tr_b16 v[68:69], v190 offset:12288\n"
    "v_lshlrev_b32 v182, 16, v209\n"
    "v_and_b32 v183, s50, v209\n"
    "v_lshlrev_b32 v184, 16, v210\n"
    "ds_read_b64_tr_b16 v[70:71], v190 offset:13312\n"
    "v_and_b32 v185, s50, v210\n"
    "v_lshlrev_b32 v186, 16, v211\n"
    "v_and_b32 v187, s50, v211\n"
    "ds_read_b64_tr_b16 v[76:77], v190 offset:14336\n"
    "ds_read_b64_tr_b16 v[78:79], v190 offset:15360\n"
    "s_cmp_lg_u32 s30, 0\n"
    "s_cbranch_scc1 .Lhc46\n"
    "s_waitcnt lgkmcnt(0)\n"
    "s_sub_u32 s54, s10, 400\n"
    "s_movk_i32 s70, 400\n"
    "s_sub_u32 s55, s80, s54\n"
    "s_max_u32 s70, s70, s55\n"
    "s_sub_u32 s55, s81, s54\n"
    "s_max_u32 s70, s70, s55\n"
    "s_sub_u32 s55, s82, s54\n"
    "s_max_u32 s70, s70, s55\n"
    "s_sub_u32 s55, s83, s54\n"
    "s_max_u32 s70, s70, s55\n"
    "s_sub_u32 s55, s84, s54\n"
    "s_max_u32 s70, s70, s55\n"
    "s_sub_u32 s55, s85, s54\n"
    "s_max_u32 s70, s70, s55\n"
    "s_sub_u32 s55, s86, s54\n"
    "s_max_u32 s70, s70, s55\n"
    "s_sub_u32 s55, s87, s54\n"
    "s_max_u32 s70, s70, s55\n"
    "s_sub_u32 s70, s70, 400\n"
    "s_cmp_gt_u32 s70, 400\n"
    "s_cbranch_scc1 .Lhr47\n"
    ".Lhb48:\n"
    "s_add_u32 s70, s70, s10\n"
    "s_add_u32 s70, s70, 95\n"
    "s_mov_b32 s56, 64\n"
    ".Lwu50:\n"
    "s_memrealtime s[54:55]\n"
    "s_sub_u32 s56, s56, 1\n"
    "s_waitcnt lgkmcnt(0)\n"
    "s_sub_u32 s54, s70, s54\n"
    "s_min_i32 s54, s54, s56\n"
    "s_cmp_gt_i32 s54, 0\n"
    "s_cbranch_scc1 .Lwu50\n"
    ".Lhc46:\n"
    "s_cmp_eq_u32 s30, 3\n"
    "s_cbranch_scc1 .Lw3\n"
    "s_waitcnt lgkmcnt(0)\n"
    "s_barrier\n"
    "s_memrealtime s[10:11]\n"
    ".Lw3b:\n"
    "ds_read_b128 v[148:151], v193 offset:0\n"
    "ds_read_b128 v[152:155], v193 offset:16\n"
    "ds_read_b128 v[156:159], v193 offset:256\n"
    "ds_read_b128 v[160:163], v193 offset:272\n"
    "ds_read_b128 v[164:167], v193 offset:512\n"
    "ds_read_b128 v[168:171], v193 offset:528\n"
    "ds_read_b128 v[172:175], v193 offset:768\n"
    "ds_read_b128 v[176:179], v193 offset:784\n"
    "global_load_dwordx4 v[80:83], v191, s[16:17] offset:-4096 sc1\n"
    "global_load_dwordx4 v[84:87], v191, s[16:17] offset:-3072 sc1\n"
    "global_load_dwordx4 v[88:91], v191, s[16:17] offset:-2048 sc1\n"
    "global_load_dwordx4 v[92:95], v191, s[16:17] offset:-1024 sc1\n"
    "global_load_dwordx4 v[96:99], v191, s[16:17] offset:0 sc1\n"
    "global_load_dwordx4 v[100:103], v191, s[16:17] offset:1024 sc1\n"
    "global_load_dwordx4 v[104:107], v191, s[16:17] offset:2048 sc1\n"
    "global_load_dwordx4 v[108:111], v191, s[16:17] offset:3072 sc1\n"
    "s_mov_b32 exec_lo, 0xff00ff\n"
    "s_mov_b32 exec_hi, 0xff00ff\n"
    "global_load_dwordx2 v[144:145], v192, s[16:17] sc1\n"
    "s_mov_b64 exec, -1\n"
    "s_lshl1_add_u32 s54, s45, s44\n"
    "s_lshl_b32 s54, s54, 13\n"
    "v_and_b32 v240, 31, v1\n"
    "v_lshl_add_u32 v240, v240, 8, s54\n"
    "v_lshrrev_b32 v241, 5, v1\n"
    "v_xor_b32 v241, v241, v239\n"
    "v_lshl_add_u32 v146, v241, 4, v240\n"
    "v_xor_b32 v147, 32, v146\n"
    "v_xor_b32 v188, 64, v146\n"
    "v_xor_b32 v189, 96, v146\n"
    "v_lshrrev_b32 v240, 3, v0\n"
    "v_and_b32 v241, 7, v240\n"
    "v_lshlrev_b32 v242, 1, v239\n"
    "v_xor_b32 v241, v241, v242\n"
    "v_lshlrev_b32 v241, 4, v241\n"
    "v_lshl_add_u32 v196, v240, 8, v241\n"
    "v_xor_b32 v197, 16, v196\n"
    "s_lshl3_add_u32 s54, s36, s37\n"
    "s_lshl3_add_u32 s55, s54, s30\n"
    "s_lshl_b32 s55, s55, 7\n"
    "s_add_u32 s55, s55, 851968\n"
    "v_lshl_add_u32 v194, v1, 1, s55\n"
    "s_mov_b32 s64, 0xcccccccc\n"
    "s_mov_b32 s65, 0xcccccccc\n"
    "s_mov_b32 s66, 0xf0f0f0f0\n"
    "s_mov_b32 s67, 0xf0f0f0f0\n"
    "s_lshl3_add_u32 s98, s31, s37\n"
    "s_cmp_ge_u32 s32, 24\n"
    "s_cselect_b32 s54, s98, 56\n"
    "s_cmp_lt_u32 s54, 56\n"
    "s_cselect_b32 s99, 1, 0\n"
    "v_lshrrev_b32 v240, 2, v1\n"
    "v_lshl_add_u32 v240, s30, 6, v240\n"
    "v_mul_u32_u24 v240, 3584, v240\n"
    "v_and_b32 v241, 0x30, v2\n"
    "s_lshl_b32 s54, s98, 6\n"
    "v_add3_u32 v229, v240, v241, s54\n"
    "s_lshl_b32 s7, s30, 13\n"
    "s_add_u32 s7, s7, 65536\n"
    "v_lshlrev_b32 v216, 16, v204\n"
    "v_and_b32 v217, s50, v204\n"
    "v_lshlrev_b32 v218, 16, v205\n"
    "v_and_b32 v219, s50, v205\n"
    "v_lshlrev_b32 v220, 16, v206\n"
    "v_and_b32 v221, s50, v206\n"
    "v_lshlrev_b32 v222, 16, v207\n"
    "v_and_b32 v223, s50, v207\n"
    "s_waitcnt lgkmcnt(0)\n"
    "s_cmp_lg_u32 s30, 3\n"
    "s_cbranch_scc1 .Lbc52\n"
    "v_mul_f32 v226, v156, v180\n"
    "v_mul_f32 v224, v164, v180\n"
    "v_fmac_f32 v226, v157, v181\n"
    "v_fmac_f32 v224, v165, v181\n"
    "v_fmac_f32 v226, v158, v182\n"
    "v_fmac_f32 v224, v166, v182\n"
    "v_fmac_f32 v226, v159, v183\n"
    "v_fmac_f32 v224, v167, v183\n"
    "v_fmac_f32 v226, v160, v184\n"
    "v_fmac_f32 v224, v168, v184\n"
    "v_fmac_f32 v226, v161, v185\n"
    "v_fmac_f32 v224, v169, v185\n"
    "v_fmac_f32 v226, v162, v186\n"
    "v_fmac_f32 v224, v170, v186\n"
    "v_fmac_f32 v226, v163, v187\n"
    "v_fmac_f32 v224, v171, v187\n"
    "v_mov_b32 v225, s38\n"
    "v_mov_b32 v227, s38\n"
    "v_add_f32_dpp v224, v224, v224 quad_perm:[1,0,3,2] row_mask:0xf bank_mask:0xf bound_ctrl:1\n"
    "v_add_f32_dpp v226, v226, v226 quad_perm:[1,0,3,2] row_mask:0xf bank_mask:0xf bound_ctrl:1\n"
    "s_nop 0\n"
    "v_add_f32_dpp v224, v224, v224 quad_perm:[2,3,0,1] row_mask:0xf bank_mask:0xf bound_ctrl:1\n"
    "v_add_f32_dpp v226, v226, v226 quad_perm:[2,3,0,1] row_mask:0xf bank_mask:0xf bound_ctrl:1\n"
    "s_nop 0\n"
    "v_add_f32_dpp v224, v224, v224 row_half_mirror row_mask:0xf bank_mask:0xf bound_ctrl:1\n"
    "v_add_f32_dpp v226, v226, v226 row_half_mirror row_mask:0xf bank_mask:0xf bound_ctrl:1\n"
    "s_lshl3_add_u32 s54, s36, s37\n"
    "s_lshl_b32 s54, s54, 7\n"
    "s_add_u32 s54, s54, 163840\n"
    "v_add_u32 v195, s54, v2\n"
    ".Lbc52:\n"
    "v_mul_f32 v180, v164, v180\n"
    "v_mul_f32 v181, v165, v181\n"
    "v_mul_f32 v182, v166, v182\n"
    "v_mul_f32 v183, v167, v183\n"
    "v_mul_f32 v184, v168, v184\n"
    "v_mul_f32 v185, v169, v185\n"
    "v_mul_f32 v186, v170, v186\n"
    "v_mul_f32 v187, v171, v187\n"
    ".Log53:\n"
    "s_waitcnt vmcnt(8)\n"
    "v_mfma_f32_32x32x16_bf16 v[112:127], v[16:19], v[80:83], 0\n"
    "v_add3_u32 v238, v80, v81, v82\n"
    "v_add_u32 v238, v238, v83\n"
    "s_waitcnt vmcnt(7)\n"
    "v_mfma_f32_32x32x16_bf16 v[112:127], v[24:27], v[84:87], v[112:127]\n"
    "v_add3_u32 v238, v238, v84, v85\n"
    "v_add3_u32 v238, v238, v86, v87\n"
    "s_waitcnt vmcnt(6)\n"
    "v_mfma_f32_32x32x16_bf16 v[112:127], v[32:35], v[88:91], v[112:127]\n"
    "v_add3_u32 v238, v238, v88, v89\n"
    "v_add3_u32 v238, v238, v90, v91\n"
    "s_waitcnt vmcnt(5)\n"
    "v_mfma_f32_32x32x16_bf16 v[112:127], v[40:43], v[92:95], v[112:127]\n"
    "v_add3_u32 v238, v238, v92, v93\n"
    "v_add3_u32 v238, v238, v94, v95\n"
    "s_waitcnt vmcnt(4)\n"
    "v_mfma_f32_32x32x16_bf16 v[112:127], v[48:51], v[96:99], v[112:127]\n"
    "v_add3_u32 v238, v238, v96, v97\n"
    "v_add3_u32 v238, v238, v98, v99\n"
    "s_waitcnt vmcnt(3)\n"
    "v_mfma_f32_32x32x16_bf16 v[112:127], v[56:59], v[100:103], v[112:127]\n"
    "v_add3_u32 v238, v238, v100, v101\n"
    "v_add3_u32 v238, v238, v102, v103\n"
    "s_waitcnt vmcnt(2)\n"
    "v_mfma_f32_32x32x16_bf16 v[112:127], v[64:67], v[104:107], v[112:127]\n"
    "v_add3_u32 v238, v238, v104, v105\n"
    "v_add3_u32 v238, v238, v106, v107\n"
    "s_waitcnt vmcnt(1)\n"
    "v_mfma_f32_32x32x16_bf16 v[112:127], v[72:75], v[108:111], v[112:127]\n"
    "v_add3_u32 v238, v238, v108, v109\n"
    "v_add3_u32 v238, v238, v110, v111\n"
    "s_waitcnt vmcnt(0)\n"
    "v_mfma_f32_32x32x16_bf16 v[128:143], v[20:23], v[80:83], 0\n"
    "v_sub_u32 v238, v238, v144\n"
    "s_nop 1\n"
    "v_add_u32_dpp v238, v238, v238 row_ror:8 row_mask:0xf bank_mask:0xf bound_ctrl:1\n"
    "v_mfma_f32_32x32x16_bf16 v[128:143], v[28:31], v[84:87], v[128:143]\n"
    "s_nop 0\n"
    "v_add_u32_dpp v238, v238, v238 row_ror:4 row_mask:0xf bank_mask:0xf bound_ctrl:1\n"
    "s_nop 1\n"
    "v_add_u32_dpp v238, v238, v238 row_ror:2 row_mask:0xf bank_mask:0xf bound_ctrl:1\n"
    "v_mfma_f32_32x32x16_bf16 v[128:143], v[36:39], v[88:91], v[128:143]\n"
    "s_nop 0\n"
    "v_add_u32_dpp v238, v238, v238 row_ror:1 row_mask:0xf bank_mask:0xf bound_ctrl:1\n"
    "v_bitop3_b32 v238, v238, v145, s38 bitop3:0xf6\n"
    "v_mfma_f32_32x32x16_bf16 v[128:143], v[44:47], v[92:95], v[128:143]\n"
    "v_cmp_ne_u32 vcc, 0, v238\n"
    "v_mfma_f32_32x32x16_bf16 v[128:143], v[52:55], v[96:99], v[128:143]\n"
    "ds_write_b128 v146, v[112:115]\n"
    "v_mfma_f32_32x32x16_bf16 v[128:143], v[60:63], v[100:103], v[128:143]\n"
    "ds_write_b128 v147, v[116:119]\n"
    "v_mfma_f32_32x32x16_bf16 v[128:143], v[68:71], v[104:107], v[128:143]\n"
    "ds_write_b128 v188, v[120:123]\n"
    "v_mfma_f32_32x32x16_bf16 v[128:143], v[76:79], v[108:111], v[128:143]\n"
    "ds_write_b128 v189, v[124:127]\n"
    "s_and_b64 vcc, vcc, s[52:53]\n"
    "s_cbranch_scc1 .Lor54\n"
    "s_nop 8\n"
    "ds_write_b128 v146, v[128:131] offset:128\n"
    "ds_write_b128 v147, v[132:135] offset:128\n"
    "ds_write_b128 v188, v[136:139] offset:128\n"
    "ds_write_b128 v189, v[140:143] offset:128\n"
    "s_cmp_ge_u32 s32, 24\n"
    "s_cbranch_scc1 .Ldp51\n"
    "s_lshl3_add_u32 s54, s32, s30\n"
    "s_mul_i32 s54, s54, 11\n"
    "s_sub_u32 s54, s54, 0\n"
    "s_min_u32 s54, s54, 2037\n"
    "s_min_u32 s55, s31, 6\n"
    "s_lshl_b32 s55, s55, 11\n"
    "s_add_u32 s54, s54, s55\n"
    "s_lshl_b32 s54, s54, 7\n"
    "s_add_u32 s62, s28, s54\n"
    "s_addc_u32 s63, s29, 0\n"
    "v_lshlrev_b32 v243, 3, v2\n"
    "s_mov_b64 exec, 0x7ff\n"
    "global_load_dword v244, v243, s[62:63]\n"
    "s_mov_b64 exec, -1\n"
    ".Ldp51:\n"
    "s_cmp_ge_u32 s30, 4\n"
    "s_cbranch_scc1 .Lmd56\n"
    "s_cmp_eq_u32 s99, 0\n"
    "s_cbranch_scc1 .Lmd56\n"
    "s_add_u32 m0, s7, 0\n"
    "v_add_u32 v246, 0, v229\n"
    "global_load_lds_dwordx4 v246, s[24:25]\n"
    "s_add_u32 m0, s7, 4096\n"
    "v_add_u32 v247, 0, v229\n"
    "global_load_lds_dwordx4 v247, s[26:27]\n"
    "s_add_u32 m0, s7, 1024\n"
    "v_add_u32 v246, 57344, v229\n"
    "global_load_lds_dwordx4 v246, s[24:25]\n"
    "s_add_u32 m0, s7, 5120\n"
    "v_add_u32 v247, 57344, v229\n"
    "global_load_lds_dwordx4 v247, s[26:27]\n"
    "s_add_u32 m0, s7, 2048\n"
    "v_add_u32 v246, 114688, v229\n"
    "global_load_lds_dwordx4 v246, s[24:25]\n"
    "s_add_u32 m0, s7, 6144\n"
    "v_add_u32 v247, 114688, v229\n"
    "global_load_lds_dwordx4 v247, s[26:27]\n"
    "s_add_u32 m0, s7, 3072\n"
    "v_add_u32 v246, 172032, v229\n"
    "global_load_lds_dwordx4 v246, s[24:25]\n"
    "s_add_u32 m0, s7, 7168\n"
    "v_add_u32 v247, 172032, v229\n"
    "global_load_lds_dwordx4 v247, s[26:27]\n"
    ".Lmd56:\n"
    "s_cmp_lg_u32 s30, 3\n"
    "s_cbranch_scc1 .Lbc57\n"
    "s_mov_b64 exec, 0xff\n"
    "global_store_dwordx4 v195, v[224:227], s[16:17] sc1\n"
    "s_mov_b64 exec, -1\n"
    ".Lbc57:\n"
    "s_cmp_lg_u32 s30, 0\n"
    "s_cbranch_scc1 .Lh4\n"
    "s_lshl_b32 s58, s36, 10\n"
    "s_add_u32 s58, s58, 1179648\n"
    "s_add_u32 s62, s16, s58\n"
    "s_addc_u32 s63, s17, 0\n"
    "s_load_dword s80, s[62:63], 0x0\n"
    "s_load_dword s81, s[62:63], 0x80\n"
    "s_load_dword s82, s[62:63], 0x100\n"
    "s_load_dword s83, s[62:63], 0x180\n"
    "s_load_dword s84, s[62:63], 0x200\n"
    "s_load_dword s85, s[62:63], 0x280\n"
    "s_load_dword s86, s[62:63], 0x300\n"
    "s_load_dword s87, s[62:63], 0x380\n"
    "s_waitcnt lgkmcnt(0)\n"
    "s_sub_u32 s54, s10, 400\n"
    "s_movk_i32 s71, 400\n"
    "s_sub_u32 s55, s80, s54\n"
    "s_max_u32 s71, s71, s55\n"
    "s_sub_u32 s55, s81, s54\n"
    "s_max_u32 s71, s71, s55\n"
    "s_sub_u32 s55, s82, s54\n"
    "s_max_u32 s71, s71, s55\n"
    "s_sub_u32 s55, s83, s54\n"
    "s_max_u32 s71, s71, s55\n"
    "s_sub_u32 s55, s84, s54\n"
    "s_max_u32 s71, s71, s55\n"
    "s_sub_u32 s55, s85, s54\n"
    "s_max_u32 s71, s71, s55\n"
    "s_sub_u32 s55, s86, s54\n"
    "s_max_u32 s71, s71, s55\n"
    "s_sub_u32 s55, s87, s54\n"
    "s_max_u32 s71, s71, s55\n"
    "s_sub_u32 s71, s71, 400\n"
    "s_cmp_gt_u32 s71, 400\n"
    "s_cbranch_scc1 .Lhr58\n"
    ".Lhb59:\n"
    ".Lh4:\n"
    "s_waitcnt lgkmcnt(0)\n"
    "s_barrier\n"
    "ds_read_b128 v[80:83], v196 offset:0\n"
    "ds_read_b128 v[88:91], v196 offset:16384\n"
    "ds_read_b128 v[96:99], v196 offset:32768\n"
    "ds_read_b128 v[104:107], v196 offset:49152\n"
    "ds_read_b128 v[84:87], v197 offset:0\n"
    "ds_read_b128 v[92:95], v197 offset:16384\n"
    "ds_read_b128 v[100:103], v197 offset:32768\n"
    "ds_read_b128 v[108:111], v197 offset:49152\n"
    "s_cmp_lt_u32 s30, 4\n"
    "s_cbranch_scc1 .Lmd61\n"
    "s_cmp_eq_u32 s99, 0\n"
    "s_cbranch_scc1 .Lmd61\n"
    "s_add_u32 m0, s7, 0\n"
    "v_add_u32 v246, 0, v229\n"
    "global_load_lds_dwordx4 v246, s[24:25]\n"
    "s_add_u32 m0, s7, 4096\n"
    "v_add_u32 v247, 0, v229\n"
    "global_load_lds_dwordx4 v247, s[26:27]\n"
    "s_add_u32 m0, s7, 1024\n"
    "v_add_u32 v246, 57344, v229\n"
    "global_load_lds_dwordx4 v246, s[24:25]\n"
    "s_add_u32 m0, s7, 5120\n"
    "v_add_u32 v247, 57344, v229\n"
    "global_load_lds_dwordx4 v247, s[26:27]\n"
    ".Lmd61:\n"
    "s_waitcnt lgkmcnt(6)\n"
    "v_pk_add_f32 v[80:81], v[80:81], v[88:89]\n"
    "v_pk_add_f32 v[82:83], v[82:83], v[90:91]\n"
    "s_waitcnt lgkmcnt(4)\n"
    "v_pk_add_f32 v[96:97], v[96:97], v[104:105]\n"
    "v_pk_add_f32 v[98:99], v[98:99], v[106:107]\n"
    "v_pk_add_f32 v[80:81], v[80:81], v[96:97]\n"
    "v_pk_add_f32 v[82:83], v[82:83], v[98:99]\n"
    "v_pk_fma_f32 v[216:217], v[148:149], v[80:81], v[216:217]\n"
    "v_pk_fma_f32 v[218:219], v[150:151], v[82:83], v[218:219]\n"
    "s_waitcnt lgkmcnt(2)\n"
    "v_pk_add_f32 v[84:85], v[84:85], v[92:93]\n"
    "v_pk_add_f32 v[86:87], v[86:87], v[94:95]\n"
    "s_waitcnt lgkmcnt(0)\n"
    "v_pk_add_f32 v[100:101], v[100:101], v[108:109]\n"
    "v_pk_add_f32 v[102:103], v[102:103], v[110:111]\n"
    "v_pk_add_f32 v[84:85], v[84:85], v[100:101]\n"
    "v_pk_add_f32 v[86:87], v[86:87], v[102:103]\n"
    "v_pk_fma_f32 v[220:221], v[152:153], v[84:85], v[220:221]\n"
    "v_pk_fma_f32 v[222:223], v[154:155], v[86:87], v[222:223]\n"
    "v_pk_add_f32 v[240:241], v[216:217], v[218:219]\n"
    "v_pk_mul_f32 v[242:243], v[216:217], v[216:217]\n"
    "v_pk_mul_f32 v[246:247], v[216:217], v[180:181]\n"
    "v_pk_fma_f32 v[242:243], v[218:219], v[218:219], v[242:243]\n"
    "v_pk_fma_f32 v[246:247], v[218:219], v[182:183], v[246:247]\n"
    "v_pk_add_f32 v[240:241], v[240:241], v[220:221]\n"
    "v_pk_fma_f32 v[242:243], v[220:221], v[220:221], v[242:243]\n"
    "v_pk_fma_f32 v[246:247], v[220:221], v[184:185], v[246:247]\n"
    "v_pk_add_f32 v[240:241], v[240:241], v[222:223]\n"
    "v_pk_fma_f32 v[242:243], v[222:223], v[222:223], v[242:243]\n"
    "v_pk_fma_f32 v[246:247], v[222:223], v[186:187], v[246:247]\n"
    "v_add_f32 v240, v240, v241\n"
    "v_add_f32 v242, v242, v243\n"
    "v_add_f32 v246, v246, v247\n"
    "v_add_f32_dpp v240, v240, v240 quad_perm:[1,0,3,2] row_mask:0xf bank_mask:0xf bound_ctrl:1\n"
    "v_add_f32_dpp v242, v242, v242 quad_perm:[1,0,3,2] row_mask:0xf bank_mask:0xf bound_ctrl:1\n"
    "v_add_f32_dpp v246, v246, v246 quad_perm:[1,0,3,2] row_mask:0xf bank_mask:0xf bound_ctrl:1\n"
    "v_add_f32_dpp v240, v240, v240 quad_perm:[2,3,0,1] row_mask:0xf bank_mask:0xf bound_ctrl:1\n"
    "v_add_f32_dpp v242, v242, v242 quad_perm:[2,3,0,1] row_mask:0xf bank_mask:0xf bound_ctrl:1\n"
    "v_add_f32_dpp v246, v246, v246 quad_perm:[2,3,0,1] row_mask:0xf bank_mask:0xf bound_ctrl:1\n"
    "v_add_f32_dpp v240, v240, v240 row_half_mirror row_mask:0xf bank_mask:0xf bound_ctrl:1\n"
    "v_add_f32_dpp v242, v242, v242 row_half_mirror row_mask:0xf bank_mask:0xf bound_ctrl:1\n"
    "v_add_f32_dpp v246, v246, v246 row_half_mirror row_mask:0xf bank_mask:0xf bound_ctrl:1\n"
    "v_add3_u32 v238, v240, v242, v246\n"
    "v_cndmask_b32_e64 v240, v240, v242, s[64:65]\n"
    "v_xor_b32 v238, s38, v238\n"
    "v_cndmask_b32_e64 v246, v246, v238, s[64:65]\n"
    "v_cndmask_b32_e64 v240, v240, v246, s[66:67]\n"
    "v_lshrrev_b32 v240, v2, v240\n"
    "global_store_short v194, v240, s[16:17] sc1\n"
    "s_memrealtime s[60:61]\n"
    "s_cmp_lt_u32 s30, 4\n"
    "s_cbranch_scc1 .Lmd62\n"
    "s_cmp_eq_u32 s99, 0\n"
    "s_cbranch_scc1 .Lmd62\n"
    "s_add_u32 m0, s7, 2048\n"
    "v_add_u32 v246, 114688, v229\n"
    "global_load_lds_dwordx4 v246, s[24:25]\n"
    "s_add_u32 m0, s7, 6144\n"
    "v_add_u32 v247, 114688, v229\n"
    "global_load_lds_dwordx4 v247, s[26:27]\n"
    "s_add_u32 m0, s7, 3072\n"
    "v_add_u32 v246, 172032, v229\n"
    "global_load_lds_dwordx4 v246, s[24:25]\n"
    "s_add_u32 m0, s7, 7168\n"
    "v_add_u32 v247, 172032, v229\n"
    "global_load_lds_dwordx4 v247, s[26:27]\n"
    ".Lmd62:\n"
    "v_lshrrev_b32 v234, 3, v0\n"
    "v_lshlrev_b32 v230, 4, v234\n"
    "v_add_u32 v230, 147456, v230\n"
    "s_lshl4_add_u32 s54, s36, s37\n"
    "s_lshl_b32 s55, s54, 8\n"
    "s_add_u32 s55, s55, 196608\n"
    "v_and_b32 v239, 0x70, v2\n"
    "v_add_u32 v231, s55, v239\n"
    "s_lshl_b32 s55, s54, 10\n"
    "s_add_u32 s55, s55, 1376256\n"
    "v_add_u32 v233, s55, v239\n"
    "v_lshrrev_b32 v240, 3, v1\n"
    "v_lshl_add_u32 v232, v240, 10, v239\n"
    "s_lshl_b32 s54, s98, 15\n"
    "s_lshl_b32 s55, s30, 7\n"
    "s_add_u32 s54, s54, s55\n"
    "v_add_u32 v232, s54, v232\n"
    "v_mov_b32 v250, 148480\n"
    "s_cmp_lg_u32 s30, 0\n"
    "s_cbranch_scc1 .Lb8b\n"
    "s_lshl_b32 s57, s37, 5\n"
    "s_add_u32 s57, s57, s36\n"
    "s_lshl_b32 s57, s57, 7\n"
    "s_add_u32 s54, s57, 131072\n"
    "s_add_u32 s57, s57, 1146880\n"
    "v_add_u32 v62, s54, v2\n"
    "s_waitcnt lgkmcnt(0)\n"
    "s_add_u32 s70, s60, s71\n"
    "s_add_u32 s70, s70, 38\n"
    "v_mov_b32 v200, s70\n"
    "v_mov_b32 v201, s38\n"
    "v_lshlrev_b32 v198, 2, v10\n"
    "s_add_u32 s62, s16, s57\n"
    "s_addc_u32 s63, s17, 0\n"
    "s_mov_b64 exec, 0xffff\n"
    "global_store_dwordx2 v198, v[200:201], s[62:63] sc1\n"
    "s_mov_b64 exec, -1\n"
    "s_lshl_b32 s54, s36, 13\n"
    "s_add_u32 s54, s54, 856064\n"
    "v_add_u32 v60, s54, v2\n"
    "s_lshl_b32 s54, s36, 10\n"
    "s_add_u32 s54, s54, 163840\n"
    "v_lshl_add_u32 v61, v1, 7, s54\n"
    "v_add_u32 v63, 147456, v2\n"
    "s_lshl_b32 s54, s36, 7\n"
    "v_add_u32 v76, s54, v1\n"
    "v_mov_b32 v49, s38\n"
    "v_mov_b32 v51, s38\n"
    "v_mov_b32 v57, s38\n"
    "v_mov_b32 v59, s38\n"
    "s_mov_b32 s59, 0xfffff000\n"
    "s_mov_b32 s74, 1\n"
    "v_mov_b32 v198, v61\n"
    "s_cmp_eq_u32 s99, 0\n"
    "s_cbranch_scc1 .Lm1\n"
    "s_lshl_b32 s54, s31, 12\n"
    "s_add_u32 s54, s54, 131072\n"
    "v_lshl_add_u32 v84, v1, 7, s54\n"
    "v_add_u32 v198, 1015808, v84\n"
    "v_mov_b32 v75, s38\n"
    "s_lshl_b32 s54, s30, 12\n"
    "v_lshrrev_b32 v64, 4, v1\n"
    "v_bfe_u32 v238, v1, 2, 2\n"
    "v_lshl_add_u32 v238, v64, 3, v238\n"
    "v_and_b32 v239, 3, v1\n"
    "v_lshlrev_b32 v239, 3, v239\n"
    "v_lshl_add_u32 v72, v238, 6, v239\n"
    "v_add_u32 v72, s7, v72\n"
    "v_lshl_add_u32 v73, v238, 7, v239\n"
    "v_add_u32 v73, s54, v73\n"
    "v_and_b32 v238, 31, v1\n"
    "v_mul_u32_u24 v238, 80, v238\n"
    "v_lshrrev_b32 v239, 5, v1\n"
    "v_lshlrev_b32 v239, 5, v239\n"
    "s_lshl2_add_u32 s54, s30, 151616\n"
    "v_add3_u32 v65, v238, v239, s54\n"
    "v_mul_u32_u24 v66, 80, v1\n"
    "v_add_u32 v66, 151616, v66\n"
    "s_lshl_b32 s54, s30, 7\n"
    "s_add_u32 s54, s54, 150592\n"
    "v_lshl_add_u32 v67, v1, 1, s54\n"
    "v_and_b32 v238, 1, v1\n"
    "v_lshlrev_b32 v239, 6, v238\n"
    "v_lshl_add_u32 v239, v64, 4, v239\n"
    "v_add_u32 v68, s54, v239\n"
    "v_lshlrev_b32 v239, 10, v238\n"
    "v_lshl_add_u32 v239, v64, 4, v239\n"
    "s_add_u32 s54, s54, -2048\n"
    "v_add_u32 v70, s54, v239\n"
    "s_lshl_b32 s54, s98, 12\n"
    "s_lshl_b32 s55, s30, 9\n"
    "s_add_u32 s54, s54, s55\n"
    "s_add_u32 s54, s54, 1900544\n"
    "v_lshl_add_u32 v69, v1, 3, s54\n"
    "v_and_b32 v238, 0x70, v2\n"
    "v_lshl_add_u32 v238, v64, 7, v238\n"
    "v_and_b32 v239, 8, v1\n"
    "s_mov_b32 s54, 148544\n"
    "v_add3_u32 v71, v238, v239, s54\n"
    "s_mov_b32 s80, 0xffff0000\n"
    "s_mov_b32 s81, 0xffff0000\n"
    "s_mov_b32 s82, 0\n"
    "s_mov_b32 s83, -1\n"
    "s_mov_b32 s74, -1\n"
    ".Lm1:\n"
    "s_mov_b32 s56, 64\n"
    ".Lwu63:\n"
    "s_memrealtime s[54:55]\n"
    "s_sub_u32 s56, s56, 1\n"
    "s_waitcnt lgkmcnt(0)\n"
    "s_sub_u32 s54, s70, s54\n"
    "s_min_i32 s54, s54, s56\n"
    "s_cmp_gt_i32 s54, 0\n"
    "s_cbranch_scc1 .Lwu63\n"
    "s_mov_b64 exec, 0xff\n"
    "global_load_dwordx4 v[48:51], v61, s[16:17] sc1\n"
    "s_mov_b64 exec, -1\n"
    "global_load_dwordx4 v[16:19], v60, s[16:17] offset:-4096 sc1\n"
    "global_load_dwordx4 v[20:23], v60, s[16:17] offset:-3072 sc1\n"
    "global_load_dwordx4 v[24:27], v60, s[16:17] offset:-2048 sc1\n"
    "global_load_dwordx4 v[28:31], v60, s[16:17] offset:-1024 sc1\n"
    "global_load_dwordx4 v[32:35], v60, s[16:17] offset:0 sc1\n"
    "global_load_dwordx4 v[36:39], v60, s[16:17] offset:1024 sc1\n"
    "global_load_dwordx4 v[40:43], v60, s[16:17] offset:2048 sc1\n"
    "global_load_dwordx4 v[44:47], v60, s[16:17] offset:3072 sc1\n"
    "s_mov_b32 exec_lo, s74\n"
    "s_mov_b32 exec_hi, 0\n"
    "global_load_dwordx2 v[74:75], v198, s[16:17] sc1\n"
    "s_mov_b64 exec, -1\n"
    "s_barrier\n"
    ".Lst64:\n"
    "s_sub_i32 s48, s48, 1\n"
    "s_cmp_gt_i32 s48, 0\n"
    "s_cselect_b64 s[52:53], -1, 0\n"
    "v_mov_b32 v58, 0xff800000\n"
    "s_waitcnt vmcnt(9)\n"
    "v_xor_b32 v77, s38, v49\n"
    "v_bitop3_b32 v77, v77, v51, s38 bitop3:0xf6\n"
    "v_add_f32_dpp v48, v48, v48 quad_perm:[1,0,3,2] row_mask:0xf bank_mask:0xf bound_ctrl:1\n"
    "v_add_f32_dpp v50, v50, v50 quad_perm:[1,0,3,2] row_mask:0xf bank_mask:0xf bound_ctrl:1\n"
    "s_nop 0\n"
    "v_add_f32_dpp v48, v48, v48 quad_perm:[2,3,0,1] row_mask:0xf bank_mask:0xf bound_ctrl:1\n"
    "v_add_f32_dpp v50, v50, v50 quad_perm:[2,3,0,1] row_mask:0xf bank_mask:0xf bound_ctrl:1\n"
    "s_nop 0\n"
    "v_add_f32_dpp v48, v48, v48 row_half_mirror row_mask:0xf bank_mask:0xf bound_ctrl:1\n"
    "v_add_f32_dpp v50, v50, v50 row_half_mirror row_mask:0xf bank_mask:0xf bound_ctrl:1\n"
    "v_readfirstlane_b32 s72, v48\n"
    "v_readfirstlane_b32 s73, v50\n"
    "s_waitcnt vmcnt(7)\n"
    "v_pk_add_f32 v[78:79], v[16:17], v[20:21]\n"
    "v_add_f32 v85, v18, v22\n"
    "s_waitcnt vmcnt(6)\n"
    "v_pk_add_f32 v[78:79], v[78:79], v[24:25]\n"
    "v_add_f32 v85, v85, v26\n"
    "s_waitcnt vmcnt(5)\n"
    "v_pk_add_f32 v[78:79], v[78:79], v[28:29]\n"
    "v_add_f32 v85, v85, v30\n"
    "s_waitcnt vmcnt(4)\n"
    "v_pk_add_f32 v[78:79], v[78:79], v[32:33]\n"
    "v_add_f32 v85, v85, v34\n"
    "s_waitcnt vmcnt(3)\n"
    "v_pk_add_f32 v[78:79], v[78:79], v[36:37]\n"
    "v_add_f32 v85, v85, v38\n"
    "s_waitcnt vmcnt(2)\n"
    "v_pk_add_f32 v[78:79], v[78:79], v[40:41]\n"
    "v_add_f32 v85, v85, v42\n"
    "s_waitcnt vmcnt(1)\n"
    "v_pk_add_f32 v[78:79], v[78:79], v[44:45]\n"
    "v_add_f32 v85, v85, v46\n"
    "v_mul_f32 v78, 0x3b000000, v78\n"
    "v_mul_f32 v79, 0x3b000000, v79\n"
    "v_fma_f32 v79, -v78, v78, v79\n"
    "v_max_f32 v79, 0, v79\n"
    "v_add_f32 v79, 0x3727c5ac, v79\n"
    "v_rsq_f32 v53, v79\n"
    "v_fma_f32 v85, -v78, s72, v85\n"
    "v_fma_f32 v54, v53, v85, s73\n"
    "v_and_or_b32 v56, v54, s59, v76\n"
    "v_mul_f32_e64 v52, -v78, v53\n"
    "ds_write_b128 v63, v[52:55]\n"
    "v_mov_b32_dpp v238, v56 row_ror:8 row_mask:0xf bank_mask:0xf bound_ctrl:1\n"
    "v_max_f32_dpp v58, v58, v58 row_ror:8 row_mask:0xf bank_mask:0xf bound_ctrl:1\n"
    "v_med3_f32 v58, v56, v238, v58\n"
    "v_max_f32 v56, v56, v238\n"
    "v_add3_u32 v16, v16, v17, v18\n"
    "v_bitop3_b32 v16, v16, v19, s38 bitop3:0x96\n"
    "v_add3_u32 v20, v20, v21, v22\n"
    "v_bitop3_b32 v20, v20, v23, s38 bitop3:0x96\n"
    "v_mov_b32_dpp v238, v56 row_ror:4 row_mask:0xf bank_mask:0xf bound_ctrl:1\n"
    "v_max_f32_dpp v58, v58, v58 row_ror:4 row_mask:0xf bank_mask:0xf bound_ctrl:1\n"
    "v_med3_f32 v58, v56, v238, v58\n"
    "v_max_f32 v56, v56, v238\n"
    "v_or3_b32 v77, v77, v16, v20\n"
    "v_add3_u32 v24, v24, v25, v26\n"
    "v_bitop3_b32 v24, v24, v27, s38 bitop3:0x96\n"
    "v_mov_b32_dpp v238, v56 row_ror:2 row_mask:0xf bank_mask:0xf bound_ctrl:1\n"
    "v_max_f32_dpp v58, v58, v58 row_ror:2 row_mask:0xf bank_mask:0xf bound_ctrl:1\n"
    "v_med3_f32 v58, v56, v238, v58\n"
    "v_max_f32 v56, v56, v238\n"
    "v_add3_u32 v28, v28, v29, v30\n"
    "v_bitop3_b32 v28, v28, v31, s38 bitop3:0x96\n"
    "v_or3_b32 v77, v77, v24, v28\n"
    "v_mov_b32_dpp v238, v56 row_ror:1 row_mask:0xf bank_mask:0xf bound_ctrl:1\n"
    "v_max_f32_dpp v58, v58, v58 row_ror:1 row_mask:0xf bank_mask:0xf bound_ctrl:1\n"
    "v_med3_f32 v58, v56, v238, v58\n"
    "v_max_f32 v56, v56, v238\n"
    "v_add3_u32 v32, v32, v33, v34\n"
    "v_bitop3_b32 v32, v32, v35, s38 bitop3:0x96\n"
    "v_add3_u32 v36, v36, v37, v38\n"
    "v_bitop3_b32 v36, v36, v39, s38 bitop3:0x96\n"
    "v_mov_b32 v238, v56\n"
    "v_mov_b32 v239, v58\n"
    "v_or3_b32 v77, v77, v32, v36\n"
    "v_permlane16_swap_b32 v56, v238\n"
    "v_permlane16_swap_b32 v58, v239\n"
    "v_max_f32 v58, v58, v239\n"
    "v_med3_f32 v58, v56, v238, v58\n"
    "v_max_f32 v56, v56, v238\n"
    "v_add3_u32 v40, v40, v41, v42\n"
    "v_bitop3_b32 v40, v40, v43, s38 bitop3:0x96\n"
    "v_add3_u32 v44, v44, v45, v46\n"
    "v_bitop3_b32 v44, v44, v47, s38 bitop3:0x96\n"
    "v_mov_b32 v238, v56\n"
    "v_mov_b32 v239, v58\n"
    "v_or3_b32 v77, v77, v40, v44\n"
    "v_permlane32_swap_b32 v56, v238\n"
    "v_permlane32_swap_b32 v58, v239\n"
    "v_max_f32 v58, v58, v239\n"
    "v_med3_f32 v58, v56, v238, v58\n"
    "v_max_f32 v56, v56, v238\n"
    "v_cmp_ne_u32 vcc, 0, v77\n"
    "v_or_b32 v58, 64, v58\n"
    "s_waitcnt vmcnt(0)\n"
    "s_and_b64 vcc, vcc, s[52:53]\n"
    "s_cbranch_scc1 .Lsr65\n"
    "s_mov_b64 exec, 0xff\n"
    "global_store_dwordx4 v62, v[56:59], s[16:17] sc1\n"
    "ds_write_b128 v250, v[56:59]\n"
    "s_mov_b64 exec, -1\n"
    "s_waitcnt lgkmcnt(0)\n"
    "s_barrier\n"
    "ds_read_b128 v[80:83], v230\n"
    "ds_read_b128 v[104:107], v250\n"
    "v_cmp_ne_u32_e64 s[68:69], s38, v75\n"
    "v_readfirstlane_b32 s70, v74\n"
    "s_nop 1\n"
    "v_subrev_u32 v74, s70, v74\n"
    "s_nop 1\n"
    "v_max_i32_dpp v74, v74, v74 row_ror:8 row_mask:0xf bank_mask:0xf bound_ctrl:1\n"
    "s_nop 1\n"
    "v_max_i32_dpp v74, v74, v74 row_ror:4 row_mask:0xf bank_mask:0xf bound_ctrl:1\n"
    "s_nop 1\n"
    "v_max_i32_dpp v74, v74, v74 row_ror:2 row_mask:0xf bank_mask:0xf bound_ctrl:1\n"
    "s_nop 1\n"
    "v_max_i32_dpp v74, v74, v74 row_ror:1 row_mask:0xf bank_mask:0xf bound_ctrl:1\n"
    "s_nop 0\n"
    "v_readlane_b32 s71, v74, 0\n"
    "v_readlane_b32 s54, v74, 16\n"
    "s_max_i32 s71, s71, s54\n"
    "s_max_i32 s71, s71, 0\n"
    "s_min_i32 s71, s71, 250\n"
    "s_add_u32 s70, s70, s71\n"
    "s_add_u32 s71, s70, 60\n"
    "s_branch .Lb9r\n"
    ".Lb8b:\n"
    "s_barrier\n"
    "s_lshr_b32 s54, s32, 3\n"
    "s_lshl3_add_u32 s54, s30, s54\n"
    "s_cmp_lg_u32 s54, 9\n"
    "s_cbranch_scc1 .Lb8d\n"
    "s_lshl_b32 s54, s31, 12\n"
    "s_add_u32 s54, s54, 131072\n"
    "v_lshl_add_u32 v84, v1, 7, s54\n"
    "v_add_u32 v198, 1015808, v84\n"
    "v_mov_b32 v75, s38\n"
    "s_sleep 6\n"
    "s_mov_b32 exec_hi, 0\n"
    "global_load_dwordx2 v[74:75], v198, s[16:17] sc1\n"
    "s_mov_b32 exec_hi, -1\n"
    ".Lb8d:\n"
    "s_cmp_eq_u32 s99, 0\n"
    "s_cbranch_scc1 .Lb8c\n"
    "s_cmp_eq_u32 s99, 0\n"
    "s_cbranch_scc1 .Lmd66\n"
    "s_lshl_b32 s54, s30, 12\n"
    "s_add_u32 m0, s54, 0\n"
    "v_add_u32 v246, 0, v232\n"
    "global_load_lds_dwordx4 v246, s[28:29]\n"
    "s_lshl_b32 s54, s30, 12\n"
    "s_add_u32 m0, s54, 1024\n"
    "v_add_u32 v247, 8192, v232\n"
    "global_load_lds_dwordx4 v247, s[28:29]\n"
    "s_lshl_b32 s54, s30, 12\n"
    "s_add_u32 m0, s54, 2048\n"
    "v_add_u32 v246, 16384, v232\n"
    "global_load_lds_dwordx4 v246, s[28:29]\n"
    "s_lshl_b32 s54, s30, 12\n"
    "s_add_u32 m0, s54, 3072\n"
    "v_add_u32 v247, 24576, v232\n"
    "global_load_lds_dwordx4 v247, s[28:29]\n"
    ".Lmd66:\n"
    "s_lshl_b32 s54, s30, 12\n"
    "v_lshrrev_b32 v64, 4, v1\n"
    "v_bfe_u32 v238, v1, 2, 2\n"
    "v_lshl_add_u32 v238, v64, 3, v238\n"
    "v_and_b32 v239, 3, v1\n"
    "v_lshlrev_b32 v239, 3, v239\n"
    "v_lshl_add_u32 v72, v238, 6, v239\n"
    "v_add_u32 v72, s7, v72\n"
    "v_lshl_add_u32 v73, v238, 7, v239\n"
    "v_add_u32 v73, s54, v73\n"
    "v_and_b32 v238, 31, v1\n"
    "v_mul_u32_u24 v238, 80, v238\n"
    "v_lshrrev_b32 v239, 5, v1\n"
    "v_lshlrev_b32 v239, 5, v239\n"
    "s_lshl2_add_u32 s54, s30, 151616\n"
    "v_add3_u32 v65, v238, v239, s54\n"
    "v_mul_u32_u24 v66, 80, v1\n"
    "v_add_u32 v66, 151616, v66\n"
    "s_lshl_b32 s54, s30, 7\n"
    "s_add_u32 s54, s54, 150592\n"
    "v_lshl_add_u32 v67, v1, 1, s54\n"
    "v_and_b32 v238, 1, v1\n"
    "v_lshlrev_b32 v239, 6, v238\n"
    "v_lshl_add_u32 v239, v64, 4, v239\n"
    "v_add_u32 v68, s54, v239\n"
    "v_lshlrev_b32 v239, 10, v238\n"
    "v_lshl_add_u32 v239, v64, 4, v239\n"
    "s_add_u32 s54, s54, -2048\n"
    "v_add_u32 v70, s54, v239\n"
    "s_lshl_b32 s54, s98, 12\n"
    "s_lshl_b32 s55, s30, 9\n"
    "s_add_u32 s54, s54, s55\n"
    "s_add_u32 s54, s54, 1900544\n"
    "v_lshl_add_u32 v69, v1, 3, s54\n"
    "v_and_b32 v238, 0x70, v2\n"
    "v_lshl_add_u32 v238, v64, 7, v238\n"
    "v_and_b32 v239, 8, v1\n"
    "s_mov_b32 s54, 148544\n"
    "v_add3_u32 v71, v238, v239, s54\n"
    "s_mov_b32 s80, 0xffff0000\n"
    "s_mov_b32 s81, 0xffff0000\n"
    "s_mov_b32 s82, 0\n"
    "s_mov_b32 s83, -1\n"
    ".Lb8c:\n"
    "s_waitcnt lgkmcnt(0)\n"
    "s_barrier\n"
    "ds_read_b128 v[80:83], v230\n"
    "ds_read_b128 v[104:107], v250\n"
    ".Lb9r:\n"
    "v_mov_b32 v97, s38\n"
    "v_mov_b32 v99, s38\n"
    "v_mov_b32 v101, s38\n"
    "v_mov_b32 v103, s38\n"
    "s_waitcnt lgkmcnt(1)\n"
    "v_pk_fma_f32 v[88:89], v[216:217], v[80:81], v[80:81] op_sel:[0,1,0] op_sel_hi:[1,1,0]\n"
    "v_pk_fma_f32 v[90:91], v[218:219], v[80:81], v[80:81] op_sel:[0,1,0] op_sel_hi:[1,1,0]\n"
    "v_pk_fma_f32 v[92:93], v[220:221], v[80:81], v[80:81] op_sel:[0,1,0] op_sel_hi:[1,1,0]\n"
    "v_pk_fma_f32 v[94:95], v[222:223], v[80:81], v[80:81] op_sel:[0,1,0] op_sel_hi:[1,1,0]\n"
    "s_waitcnt lgkmcnt(0)\n"
    "v_and_b32 v104, 63, v104\n"
    "v_and_b32 v106, 63, v106\n"
    "v_pk_fma_f32 v[88:89], v[88:89], v[164:165], v[156:157]\n"
    "v_pk_fma_f32 v[90:91], v[90:91], v[166:167], v[158:159]\n"
    "v_pk_fma_f32 v[92:93], v[92:93], v[168:169], v[160:161]\n"
    "v_pk_fma_f32 v[94:95], v[94:95], v[170:171], v[162:163]\n"
    "v_cmp_eq_u32_e64 s[78:79], v104, v234\n"
    "v_cmp_eq_u32 vcc, v106, v234\n"
    "v_pk_fma_f32 v[216:217], v[172:173], v[88:89], v[216:217]\n"
    "v_pk_fma_f32 v[218:219], v[174:175], v[90:91], v[218:219]\n"
    "v_pk_fma_f32 v[220:221], v[176:177], v[92:93], v[220:221]\n"
    "v_pk_fma_f32 v[222:223], v[178:179], v[94:95], v[222:223]\n"
    "s_or_b64 s[78:79], s[78:79], vcc\n"
    "s_cbranch_scc0 .Lcb68\n"
    "v_cvt_pk_bf16_f32 v96, v88, v89\n"
    "v_cvt_pk_bf16_f32 v98, v90, v91\n"
    "v_cvt_pk_bf16_f32 v100, v92, v93\n"
    "v_cvt_pk_bf16_f32 v102, v94, v95\n"
    "v_cndmask_b32_e64 v239, 0, 1, vcc\n"
    "v_lshl_add_u32 v231, v239, 11, v231\n"
    "v_lshl_add_u32 v233, v239, 13, v233\n"
    "s_mov_b64 exec, s[78:79]\n"
    "global_store_dwordx4 v231, v[96:99], s[16:17] sc1\n"
    "global_store_dwordx4 v231, v[100:103], s[16:17] offset:128 sc1\n"
    ".Lcb68:\n"
    "s_mov_b64 exec, -1\n"
    "s_cmp_lg_u32 s30, 0\n"
    "s_cbranch_scc1 .Lb9x\n"
    "s_cmp_eq_u32 s99, 0\n"
    "s_cbranch_scc1 .Lb9x\n"
    "s_cmp_lg_u64 s[68:69], 0\n"
    "s_cbranch_scc1 .Lcr71\n"
    ".Lch70:\n"
    "s_mov_b32 s56, 64\n"
    ".Lwu72:\n"
    "s_memrealtime s[54:55]\n"
    "s_sub_u32 s56, s56, 1\n"
    "s_waitcnt lgkmcnt(0)\n"
    "s_sub_u32 s54, s71, s54\n"
    "s_min_i32 s54, s54, s56\n"
    "s_cmp_gt_i32 s54, 0\n"
    "s_cbranch_scc1 .Lwu72\n"
    "s_mov_b32 exec_hi, 0\n"
    "global_load_dwordx4 v[144:147], v84, s[16:17] sc1\n"
    "s_mov_b32 exec_hi, -1\n"
    "s_cmp_eq_u32 s99, 0\n"
    "s_cbranch_scc1 .Lmd74\n"
    "s_lshl_b32 s54, s30, 12\n"
    "s_add_u32 m0, s54, 0\n"
    "v_add_u32 v246, 0, v232\n"
    "global_load_lds_dwordx4 v246, s[28:29]\n"
    "s_lshl_b32 s54, s30, 12\n"
    "s_add_u32 m0, s54, 1024\n"
    "v_add_u32 v247, 8192, v232\n"
    "global_load_lds_dwordx4 v247, s[28:29]\n"
    "s_lshl_b32 s54, s30, 12\n"
    "s_add_u32 m0, s54, 2048\n"
    "v_add_u32 v246, 16384, v232\n"
    "global_load_lds_dwordx4 v246, s[28:29]\n"
    "s_lshl_b32 s54, s30, 12\n"
    "s_add_u32 m0, s54, 3072\n"
    "v_add_u32 v247, 24576, v232\n"
    "global_load_lds_dwordx4 v247, s[28:29]\n"
    ".Lmd74:\n"
    "v_cvt_pk_bf16_f32 v200, v216, v217\n"
    "v_cvt_pk_bf16_f32 v201, v218, v219\n"
    "v_cvt_pk_bf16_f32 v202, v220, v221\n"
    "v_cvt_pk_bf16_f32 v203, v222, v223\n"
    "s_cmp_eq_u64 s[78:79], 0\n"
    "s_cbranch_scc1 .Lbs75\n"
    "s_mov_b64 exec, s[78:79]\n"
    "v_mul_f32 v245, 0xbfb8aa3b, v82\n"
    "v_exp_f32 v245, v245\n"
    "s_nop 0\n"
    "v_add_f32 v245, 1.0, v245\n"
    "v_rcp_f32 v245, v245\n"
    "v_mov_b32 v112, v216\n"
    "v_mov_b32 v113, s38\n"
    "v_mul_f32 v114, v172, v245\n"
    "v_mov_b32 v115, s38\n"
    "global_store_dwordx4 v233, v[112:115], s[16:17] offset:0 sc1\n"
    "v_mov_b32 v116, v217\n"
    "v_mov_b32 v117, s38\n"
    "v_mul_f32 v118, v173, v245\n"
    "v_mov_b32 v119, s38\n"
    "global_store_dwordx4 v233, v[116:119], s[16:17] offset:128 sc1\n"
    "v_mov_b32 v120, v218\n"
    "v_mov_b32 v121, s38\n"
    "v_mul_f32 v122, v174, v245\n"
    "v_mov_b32 v123, s38\n"
    "global_store_dwordx4 v233, v[120:123], s[16:17] offset:256 sc1\n"
    "v_mov_b32 v124, v219\n"
    "v_mov_b32 v125, s38\n"
    "v_mul_f32 v126, v175, v245\n"
    "v_mov_b32 v127, s38\n"
    "global_store_dwordx4 v233, v[124:127], s[16:17] offset:384 sc1\n"
    "v_mov_b32 v128, v220\n"
    "v_mov_b32 v129, s38\n"
    "v_mul_f32 v130, v176, v245\n"
    "v_mov_b32 v131, s38\n"
    "global_store_dwordx4 v233, v[128:131], s[16:17] offset:512 sc1\n"
    "v_mov_b32 v132, v221\n"
    "v_mov_b32 v133, s38\n"
    "v_mul_f32 v134, v177, v245\n"
    "v_mov_b32 v135, s38\n"
    "global_store_dwordx4 v233, v[132:135], s[16:17] offset:640 sc1\n"
    "v_mov_b32 v136, v222\n"
    "v_mov_b32 v137, s38\n"
    "v_mul_f32 v138, v178, v245\n"
    "v_mov_b32 v139, s38\n"
    "global_store_dwordx4 v233, v[136:139], s[16:17] offset:768 sc1\n"
    "v_mov_b32 v140, v223\n"
    "v_mov_b32 v141, s38\n"
    "v_mul_f32 v142, v179, v245\n"
    "v_mov_b32 v143, s38\n"
    "global_store_dwordx4 v233, v[140:143], s[16:17] offset:896 sc1\n"
    "s_mov_b64 exec, -1\n"
    "s_waitcnt vmcnt(12)\n"
    "s_branch .Lbt76\n"
    ".Lbs75:\n"
    "s_waitcnt vmcnt(4)\n"
    ".Lbt76:\n"
    "s_mov_b32 exec_hi, 0\n"
    ".Lcq73:\n"
    "v_xor_b32 v238, s38, v145\n"
    "v_bitop3_b32 v238, v238, v147, s38 bitop3:0xf6\n"
    "v_cmp_ne_u32 vcc, 0, v238\n"
    "v_max_f32 v240, v144, v146\n"
    "v_min_f32 v241, v144, v146\n"
    "s_and_b64 vcc, vcc, s[52:53]\n"
    "s_cbranch_scc1 .Lcp69\n"
    "s_mov_b32 exec_hi, -1\n"
    "v_mov_b32_dpp v238, v240 row_ror:8 row_mask:0xf bank_mask:0xf bound_ctrl:1\n"
    "v_max_f32_dpp v241, v241, v241 row_ror:8 row_mask:0xf bank_mask:0xf bound_ctrl:1\n"
    "v_med3_f32 v241, v240, v238, v241\n"
    "v_max_f32 v240, v240, v238\n"
    "s_nop 1\n"
    "v_mov_b32_dpp v238, v240 row_ror:4 row_mask:0xf bank_mask:0xf bound_ctrl:1\n"
    "v_max_f32_dpp v241, v241, v241 row_ror:4 row_mask:0xf bank_mask:0xf bound_ctrl:1\n"
    "v_med3_f32 v241, v240, v238, v241\n"
    "v_max_f32 v240, v240, v238\n"
    "s_nop 1\n"
    "v_mov_b32_dpp v238, v240 row_ror:2 row_mask:0xf bank_mask:0xf bound_ctrl:1\n"
    "v_max_f32_dpp v241, v241, v241 row_ror:2 row_mask:0xf bank_mask:0xf bound_ctrl:1\n"
    "v_med3_f32 v241, v240, v238, v241\n"
    "v_max_f32 v240, v240, v238\n"
    "s_nop 1\n"
    "v_mov_b32_dpp v238, v240 row_ror:1 row_mask:0xf bank_mask:0xf bound_ctrl:1\n"
    "v_max_f32_dpp v241, v241, v241 row_ror:1 row_mask:0xf bank_mask:0xf bound_ctrl:1\n"
    "v_med3_f32 v241, v240, v238, v241\n"
    "v_max_f32 v240, v240, v238\n"
    "v_mov_b32 v238, v240\n"
    "v_mov_b32 v239, v241\n"
    "s_nop 0\n"
    "v_permlane16_swap_b32 v240, v238\n"
    "v_permlane16_swap_b32 v241, v239\n"
    "v_max_f32 v241, v241, v239\n"
    "v_med3_f32 v241, v240, v238, v241\n"
    "v_max_f32 v240, v240, v238\n"
    "s_nop 0\n"
    "v_readfirstlane_b32 s76, v240\n"
    "v_readfirstlane_b32 s77, v241\n"
    "s_bfe_u32 s54, s76, 0x60006\n"
    "s_lshl_b32 s54, s54, 11\n"
    "s_add_u32 s54, s54, 196608\n"
    "v_add_u32 v242, s54, v2\n"
    "s_bfe_u32 s55, s77, 0x60006\n"
    "s_lshl_b32 s55, s55, 11\n"
    "s_add_u32 s55, s55, 196608\n"
    "v_add_u32 v243, s55, v2\n"
    "global_load_dwordx4 v[88:91], v242, s[16:17] offset:0 sc1\n"
    "global_load_dwordx4 v[92:95], v242, s[16:17] offset:1024 sc1\n"
    "global_load_dwordx4 v[96:99], v243, s[16:17] offset:0 sc1\n"
    "global_load_dwordx4 v[100:103], v243, s[16:17] offset:1024 sc1\n"
    "ds_read_b64_tr_b16 v[16:17], v72 offset:0\n"
    "ds_read_b64_tr_b16 v[18:19], v72 offset:256\n"
    "ds_read_b64_tr_b16 v[20:21], v72 offset:32\n"
    "ds_read_b64_tr_b16 v[22:23], v72 offset:288\n"
    "ds_read_b64_tr_b16 v[24:25], v72 offset:4096\n"
    "ds_read_b64_tr_b16 v[26:27], v72 offset:4352\n"
    "ds_read_b64_tr_b16 v[28:29], v72 offset:4128\n"
    "ds_read_b64_tr_b16 v[30:31], v72 offset:4384\n"
    "ds_read_b64_tr_b16 v[32:33], v72 offset:2048\n"
    "ds_read_b64_tr_b16 v[34:35], v72 offset:2304\n"
    "ds_read_b64_tr_b16 v[36:37], v72 offset:2080\n"
    "ds_read_b64_tr_b16 v[38:39], v72 offset:2336\n"
    "ds_read_b64_tr_b16 v[40:41], v72 offset:6144\n"
    "ds_read_b64_tr_b16 v[42:43], v72 offset:6400\n"
    "ds_read_b64_tr_b16 v[44:45], v72 offset:6176\n"
    "ds_read_b64_tr_b16 v[46:47], v72 offset:6432\n"
    "s_waitcnt vmcnt(4)\n"
    "ds_read_b64_tr_b16 v[48:49], v73 offset:0\n"
    "ds_read_b64_tr_b16 v[50:51], v73 offset:512\n"
    "ds_read_b64_tr_b16 v[52:53], v73 offset:32\n"
    "ds_read_b64_tr_b16 v[54:55], v73 offset:544\n"
    "ds_read_b64_tr_b16 v[56:57], v73 offset:64\n"
    "ds_read_b64_tr_b16 v[58:59], v73 offset:576\n"
    "ds_read_b64_tr_b16 v[60:61], v73 offset:96\n"
    "ds_read_b64_tr_b16 v[62:63], v73 offset:608\n"
    ".Lhp77:\n"
    "s_waitcnt vmcnt(3)\n"
    "v_xor_b32 v238, s38, v89\n"
    "v_bitop3_b32 v238, v238, v91, s38 bitop3:0xf6\n"
    "v_mov_b32 v89, v90\n"
    "ds_write_b64 v71, v[88:89] offset:0\n"
    "s_waitcnt vmcnt(2)\n"
    "v_bitop3_b32 v238, v238, v93, s38 bitop3:0xf6\n"
    "v_bitop3_b32 v238, v238, v95, s38 bitop3:0xf6\n"
    "v_mov_b32 v93, v94\n"
    "ds_write_b64 v71, v[92:93] offset:512\n"
    "s_waitcnt vmcnt(1)\n"
    "v_bitop3_b32 v238, v238, v97, s38 bitop3:0xf6\n"
    "v_bitop3_b32 v238, v238, v99, s38 bitop3:0xf6\n"
    "v_mov_b32 v97, v98\n"
    "ds_write_b64 v71, v[96:97] offset:1024\n"
    "s_waitcnt vmcnt(0)\n"
    "v_bitop3_b32 v238, v238, v101, s38 bitop3:0xf6\n"
    "v_bitop3_b32 v238, v238, v103, s38 bitop3:0xf6\n"
    "v_mov_b32 v101, v102\n"
    "ds_write_b64 v71, v[100:101] offset:1536\n"
    "v_cmp_ne_u32 vcc, 0, v238\n"
    "s_and_b64 vcc, vcc, s[52:53]\n"
    "s_cbranch_scc1 .Lhr78\n"
    "s_branch .Lb10b\n"
    ".Lb9x:\n"
    "v_cvt_pk_bf16_f32 v200, v216, v217\n"
    "v_cvt_pk_bf16_f32 v201, v218, v219\n"
    "v_cvt_pk_bf16_f32 v202, v220, v221\n"
    "v_cvt_pk_bf16_f32 v203, v222, v223\n"
    "s_cmp_eq_u64 s[78:79], 0\n"
    "s_cbranch_scc1 .Lbs79\n"
    "s_mov_b64 exec, s[78:79]\n"
    "v_mul_f32 v245, 0xbfb8aa3b, v82\n"
    "v_exp_f32 v245, v245\n"
    "s_nop 0\n"
    "v_add_f32 v245, 1.0, v245\n"
    "v_rcp_f32 v245, v245\n"
    "v_mov_b32 v112, v216\n"
    "v_mov_b32 v113, s38\n"
    "v_mul_f32 v114, v172, v245\n"
    "v_mov_b32 v115, s38\n"
    "global_store_dwordx4 v233, v[112:115], s[16:17] offset:0 sc1\n"
    "v_mov_b32 v116, v217\n"
    "v_mov_b32 v117, s38\n"
    "v_mul_f32 v118, v173, v245\n"
    "v_mov_b32 v119, s38\n"
    "global_store_dwordx4 v233, v[116:119], s[16:17] offset:128 sc1\n"
    "v_mov_b32 v120, v218\n"
    "v_mov_b32 v121, s38\n"
    "v_mul_f32 v122, v174, v245\n"
    "v_mov_b32 v123, s38\n"
    "global_store_dwordx4 v233, v[120:123], s[16:17] offset:256 sc1\n"
    "v_mov_b32 v124, v219\n"
    "v_mov_b32 v125, s38\n"
    "v_mul_f32 v126, v175, v245\n"
    "v_mov_b32 v127, s38\n"
    "global_store_dwordx4 v233, v[124:127], s[16:17] offset:384 sc1\n"
    "v_mov_b32 v128, v220\n"
    "v_mov_b32 v129, s38\n"
    "v_mul_f32 v130, v176, v245\n"
    "v_mov_b32 v131, s38\n"
    "global_store_dwordx4 v233, v[128:131], s[16:17] offset:512 sc1\n"
    "v_mov_b32 v132, v221\n"
    "v_mov_b32 v133, s38\n"
    "v_mul_f32 v134, v177, v245\n"
    "v_mov_b32 v135, s38\n"
    "global_store_dwordx4 v233, v[132:135], s[16:17] offset:640 sc1\n"
    "v_mov_b32 v136, v222\n"
    "v_mov_b32 v137, s38\n"
    "v_mul_f32 v138, v178, v245\n"
    "v_mov_b32 v139, s38\n"
    "global_store_dwordx4 v233, v[136:139], s[16:17] offset:768 sc1\n"
    "v_mov_b32 v140, v223\n"
    "v_mov_b32 v141, s38\n"
    "v_mul_f32 v142, v179, v245\n"
    "v_mov_b32 v143, s38\n"
    "global_store_dwordx4 v233, v[140:143], s[16:17] offset:896 sc1\n"
    "s_mov_b64 exec, -1\n"
    ".Lbs79:\n"
    "s_cmp_lg_u32 s99, 0\n"
    "s_cbranch_scc1 .Lnoout\n"
    "s_add_u32 s62, s22, s12\n"
    "s_addc_u32 s63, s23, 0\n"
    "s_not_b64 exec, s[78:79]\n"
    "global_store_dwordx4 v12, v[200:203], s[62:63] sc1\n"
    "s_mov_b64 exec, -1\n"
    ".Lnoout:\n"
    "s_lshr_b32 s54, s32, 3\n"
    "s_sub_u32 s55, s30, 1\n"
    "s_cmp_lt_u32 s55, 2\n"
    "s_cselect_b32 s54, s54, 0\n"
    "s_cmp_eq_u32 s54, 1\n"
    "s_cselect_b32 s54, 2, 0\n"
    "s_or_b32 s99, s99, s54\n"
    "s_cbranch_scc0 .Lexit\n"
    "s_cmp_eq_u32 s99, 2\n"
    "s_cbranch_scc1 .Lfin\n"
    "s_waitcnt vmcnt(0)\n"
    "ds_read_b64_tr_b16 v[16:17], v72 offset:0\n"
    "ds_read_b64_tr_b16 v[18:19], v72 offset:256\n"
    "ds_read_b64_tr_b16 v[20:21], v72 offset:32\n"
    "ds_read_b64_tr_b16 v[22:23], v72 offset:288\n"
    "ds_read_b64_tr_b16 v[24:25], v72 offset:4096\n"
    "ds_read_b64_tr_b16 v[26:27], v72 offset:4352\n"
    "ds_read_b64_tr_b16 v[28:29], v72 offset:4128\n"
    "ds_read_b64_tr_b16 v[30:31], v72 offset:4384\n"
    "ds_read_b64_tr_b16 v[32:33], v72 offset:2048\n"
    "ds_read_b64_tr_b16 v[34:35], v72 offset:2304\n"
    "ds_read_b64_tr_b16 v[36:37], v72 offset:2080\n"
    "ds_read_b64_tr_b16 v[38:39], v72 offset:2336\n"
    "ds_read_b64_tr_b16 v[40:41], v72 offset:6144\n"
    "ds_read_b64_tr_b16 v[42:43], v72 offset:6400\n"
    "ds_read_b64_tr_b16 v[44:45], v72 offset:6176\n"
    "ds_read_b64_tr_b16 v[46:47], v72 offset:6432\n"
    "ds_read_b64_tr_b16 v[48:49], v73 offset:0\n"
    "ds_read_b64_tr_b16 v[50:51], v73 offset:512\n"
    "ds_read_b64_tr_b16 v[52:53], v73 offset:32\n"
    "ds_read_b64_tr_b16 v[54:55], v73 offset:544\n"
    "ds_read_b64_tr_b16 v[56:57], v73 offset:64\n"
    "ds_read_b64_tr_b16 v[58:59], v73 offset:576\n"
    "ds_read_b64_tr_b16 v[60:61], v73 offset:96\n"
    "ds_read_b64_tr_b16 v[62:63], v73 offset:608\n"
    ".Lb10b:\n"
    "s_waitcnt lgkmcnt(0)\n"
    "s_barrier\n"
    "ds_read_b128 v[88:91], v70\n"
    "ds_read_b128 v[96:99], v70 offset:64\n"
    "s_waitcnt lgkmcnt(1)\n"
    "v_mfma_f32_16x16x32_bf16 v[104:107], v[88:91], v[16:19], 0\n"
    "v_mfma_f32_16x16x32_bf16 v[108:111], v[88:91], v[20:23], 0\n"
    "v_mfma_f32_16x16x32_bf16 v[112:115], v[88:91], v[24:27], 0\n"
    "v_mfma_f32_16x16x32_bf16 v[116:119], v[88:91], v[28:31], 0\n"
    "s_waitcnt lgkmcnt(0)\n"
    "v_mfma_f32_16x16x32_bf16 v[104:107], v[96:99], v[32:35], v[104:107]\n"
    "v_mfma_f32_16x16x32_bf16 v[108:111], v[96:99], v[36:39], v[108:111]\n"
    "v_mfma_f32_16x16x32_bf16 v[112:115], v[96:99], v[40:43], v[112:115]\n"
    "v_mfma_f32_16x16x32_bf16 v[116:119], v[96:99], v[44:47], v[116:119]\n"
    "s_nop 5\n"
    "v_cndmask_b32_e64 v104, v104, v108, s[80:81]\n"
    "v_cndmask_b32_e64 v105, v105, v109, s[80:81]\n"
    "v_cndmask_b32_e64 v112, v112, v116, s[80:81]\n"
    "v_cndmask_b32_e64 v113, v113, v117, s[80:81]\n"
    "v_cndmask_b32_e64 v238, v104, v112, s[82:83]\n"
    "v_cndmask_b32_e64 v239, v105, v113, s[82:83]\n"
    "ds_write_b32 v65, v238\n"
    "ds_write_b32 v65, v239 offset:2560\n"
    "s_waitcnt lgkmcnt(0)\n"
    "s_barrier\n"
    "ds_read_b128 v[88:91], v66 offset:0\n"
    "ds_read_b128 v[92:95], v66 offset:16\n"
    "ds_read_b128 v[96:99], v66 offset:32\n"
    "ds_read_b128 v[100:103], v66 offset:48\n"
    "s_waitcnt lgkmcnt(2)\n"
    "v_pk_add_f32 v[88:89], v[88:89], v[92:93]\n"
    "v_pk_add_f32 v[90:91], v[90:91], v[94:95]\n"
    "v_pk_add_f32 v[88:89], v[88:89], v[90:91]\n"
    "v_add_f32 v88, v88, v89\n"
    "v_mul_f32 v238, 0xbfb8aa3b, v88\n"
    "v_exp_f32 v238, v238\n"
    "s_waitcnt lgkmcnt(0)\n"
    "v_pk_add_f32 v[96:97], v[96:97], v[100:101]\n"
    "v_add_f32 v238, 1.0, v238\n"
    "v_rcp_f32 v238, v238\n"
    "v_pk_add_f32 v[98:99], v[98:99], v[102:103]\n"
    "v_pk_add_f32 v[96:97], v[96:97], v[98:99]\n"
    "v_add_f32 v96, v96, v97\n"
    "v_mul_f32 v88, v88, v238\n"
    "v_mul_f32 v88, v88, v96\n"
    "v_cvt_pk_bf16_f32 v88, v88, v88\n"
    "ds_write_b16 v67, v88\n"
    "ds_read_b128 v[92:95], v68\n"
    "v_mov_b32 v241, s38\n"
    "v_mov_b32 v243, s38\n"
    "s_waitcnt lgkmcnt(0)\n"
    "v_mfma_f32_16x16x32_bf16 v[104:107], v[92:95], v[48:51], 0\n"
    "v_mfma_f32_16x16x32_bf16 v[108:111], v[92:95], v[52:55], 0\n"
    "v_mfma_f32_16x16x32_bf16 v[112:115], v[92:95], v[56:59], 0\n"
    "v_mfma_f32_16x16x32_bf16 v[116:119], v[92:95], v[60:63], 0\n"
    "s_nop 4\n"
    "v_cvt_pk_bf16_f32 v104, v104, v105\n"
    "v_cvt_pk_bf16_f32 v108, v108, v109\n"
    "v_cvt_pk_bf16_f32 v112, v112, v113\n"
    "v_cvt_pk_bf16_f32 v116, v116, v117\n"
    "v_cndmask_b32_e64 v104, v104, v108, s[80:81]\n"
    "v_cndmask_b32_e64 v112, v112, v116, s[80:81]\n"
    "v_cndmask_b32_e64 v240, v104, v112, s[82:83]\n"
    "global_store_dwordx2 v69, v[240:241], s[16:17] sc1\n"
    "s_barrier\n"
    "s_add_u32 s62, s22, s12\n"
    "s_addc_u32 s63, s23, 0\n"
    "s_not_b64 exec, s[78:79]\n"
    "global_store_dwordx4 v12, v[200:203], s[62:63] sc1\n"
    "s_mov_b64 exec, -1\n"
    "s_branch .Lexit\n"
    ".Lfin:\n"
    "s_and_b32 s80, s32, 7\n"
    "s_lshl3_add_u32 s80, s80, s31\n"
    "v_mov_b32 v250, 148496\n"
    "s_cmp_lg_u32 s30, 1\n"
    "s_cbranch_scc1 .Lfk84\n"
    "v_lshrrev_b32 v238, 3, v1\n"
    "v_and_b32 v239, 7, v1\n"
    "v_lshlrev_b32 v238, 12, v238\n"
    "v_lshl_add_u32 v52, v239, 3, v238\n"
    "s_lshl_b32 s54, s80, 6\n"
    "s_add_u32 s54, s54, 1900544\n"
    "v_add_u32 v52, s54, v52\n"
    "s_lshr_b32 s54, s80, 3\n"
    "s_lshl_b32 s54, s54, 10\n"
    "s_and_b32 s55, s80, 7\n"
    "s_lshl4_add_u32 s54, s55, s54\n"
    "s_add_u32 s54, s54, 1376256\n"
    "v_lshl_add_u32 v53, v239, 7, s54\n"
    "s_lshl_b32 s54, s80, 4\n"
    "v_lshl_add_u32 v54, v239, 1, s54\n"
    "s_add_u32 s84, s16, 32768\n"
    "s_addc_u32 s85, s17, 0\n"
    "s_add_u32 s86, s16, 65536\n"
    "s_addc_u32 s87, s17, 0\n"
    "s_add_u32 s88, s16, 98304\n"
    "s_addc_u32 s89, s17, 0\n"
    "s_add_u32 s90, s16, 131072\n"
    "s_addc_u32 s91, s17, 0\n"
    "s_add_u32 s92, s16, 163840\n"
    "s_addc_u32 s93, s17, 0\n"
    "s_add_u32 s94, s16, 196608\n"
    "s_addc_u32 s95, s17, 0\n"
    ".Lch87:\n"
    "s_waitcnt vmcnt(0)\n"
    "v_cmp_ne_u32 vcc, s38, v75\n"
    "s_and_b64 vcc, vcc, s[52:53]\n"
    "s_cbranch_scc1 .Lcr88\n"
    "v_readfirstlane_b32 s70, v74\n"
    "s_nop 1\n"
    "v_subrev_u32 v74, s70, v74\n"
    "s_nop 1\n"
    "v_max_i32_dpp v74, v74, v74 row_ror:8 row_mask:0xf bank_mask:0xf bound_ctrl:1\n"
    "s_nop 1\n"
    "v_max_i32_dpp v74, v74, v74 row_ror:4 row_mask:0xf bank_mask:0xf bound_ctrl:1\n"
    "s_nop 1\n"
    "v_max_i32_dpp v74, v74, v74 row_ror:2 row_mask:0xf bank_mask:0xf bound_ctrl:1\n"
    "s_nop 1\n"
    "v_max_i32_dpp v74, v74, v74 row_ror:1 row_mask:0xf bank_mask:0xf bound_ctrl:1\n"
    "s_nop 0\n"
    "v_readlane_b32 s71, v74, 0\n"
    "v_readlane_b32 s54, v74, 16\n"
    "s_max_i32 s71, s71, s54\n"
    "s_max_i32 s71, s71, 0\n"
    "s_min_i32 s71, s71, 250\n"
    "s_add_u32 s70, s70, s71\n"
    "s_add_u32 s71, s70, 70\n"
    "s_mov_b32 s56, 64\n"
    ".Lwu89:\n"
    "s_memrealtime s[54:55]\n"
    "s_sub_u32 s56, s56, 1\n"
    "s_waitcnt lgkmcnt(0)\n"
    "s_sub_u32 s54, s71, s54\n"
    "s_min_i32 s54, s54, s56\n"
    "s_cmp_gt_i32 s54, 0\n"
    "s_cbranch_scc1 .Lwu89\n"
    "s_mov_b32 exec_hi, 0\n"
    ".Lcp86:\n"
    "global_load_dwordx4 v[144:147], v84, s[16:17] sc1\n"
    "s_sub_i32 s48, s48, 1\n"
    "s_cmp_gt_i32 s48, 0\n"
    "s_cselect_b64 s[52:53], -1, 0\n"
    "s_waitcnt vmcnt(0)\n"
    "v_xor_b32 v238, s38, v145\n"
    "v_bitop3_b32 v238, v238, v147, s38 bitop3:0xf6\n"
    "v_cmp_ne_u32 vcc, 0, v238\n"
    "v_max_f32 v240, v144, v146\n"
    "v_min_f32 v241, v144, v146\n"
    "s_and_b64 vcc, vcc, s[52:53]\n"
    "s_cbranch_scc1 .Lcp86\n"
    "s_mov_b32 exec_hi, -1\n"
    "v_mov_b32_dpp v238, v240 row_ror:8 row_mask:0xf bank_mask:0xf bound_ctrl:1\n"
    "v_max_f32_dpp v241, v241, v241 row_ror:8 row_mask:0xf bank_mask:0xf bound_ctrl:1\n"
    "v_med3_f32 v241, v240, v238, v241\n"
    "v_max_f32 v240, v240, v238\n"
    "s_nop 1\n"
    "v_mov_b32_dpp v238, v240 row_ror:4 row_mask:0xf bank_mask:0xf bound_ctrl:1\n"
    "v_max_f32_dpp v241, v241, v241 row_ror:4 row_mask:0xf bank_mask:0xf bound_ctrl:1\n"
    "v_med3_f32 v241, v240, v238, v241\n"
    "v_max_f32 v240, v240, v238\n"
    "s_nop 1\n"
    "v_mov_b32_dpp v238, v240 row_ror:2 row_mask:0xf bank_mask:0xf bound_ctrl:1\n"
    "v_max_f32_dpp v241, v241, v241 row_ror:2 row_mask:0xf bank_mask:0xf bound_ctrl:1\n"
    "v_med3_f32 v241, v240, v238, v241\n"
    "v_max_f32 v240, v240, v238\n"
    "s_nop 1\n"
    "v_mov_b32_dpp v238, v240 row_ror:1 row_mask:0xf bank_mask:0xf bound_ctrl:1\n"
    "v_max_f32_dpp v241, v241, v241 row_ror:1 row_mask:0xf bank_mask:0xf bound_ctrl:1\n"
    "v_med3_f32 v241, v240, v238, v241\n"
    "v_max_f32 v240, v240, v238\n"
    "v_mov_b32 v238, v240\n"
    "v_mov_b32 v239, v241\n"
    "s_nop 0\n"
    "v_permlane16_swap_b32 v240, v238\n"
    "v_permlane16_swap_b32 v241, v239\n"
    "v_max_f32 v241, v241, v239\n"
    "v_med3_f32 v241, v240, v238, v241\n"
    "v_max_f32 v240, v240, v238\n"
    "s_nop 0\n"
    "v_readfirstlane_b32 s76, v240\n"
    "v_readfirstlane_b32 s77, v241\n"
    "s_lshr_b32 s56, s80, 1\n"
    "v_readlane_b32 s57, v144, s56\n"
    "v_readlane_b32 s58, v146, s56\n"
    "s_add_u32 s81, s70, 215\n"
    "s_bitcmp1_b32 s80, 0\n"
    "s_cselect_b32 s57, s58, s57\n"
    "v_mov_b32 v242, s57\n"
    "s_mov_b64 exec, 1\n"
    "ds_write_b128 v250, v[240:243]\n"
    "s_cmp_lg_u32 s80, 0\n"
    "s_cbranch_scc1 .Lep81\n"
    "v_mov_b32 v238, 0\n"
    "v_mov_b32 v239, s38\n"
    "global_store_dword v238, v239, s[16:17] sc1\n"
    ".Lep81:\n"
    "s_mov_b64 exec, -1\n"
    "s_waitcnt lgkmcnt(0)\n"
    "s_barrier\n"
    "s_mov_b32 vcc_lo, 0xff00ff00\n"
    "s_mov_b32 vcc_hi, 0xff00ff00\n"
    "v_cndmask_b32 v240, v240, v241, vcc\n"
    "v_bfe_u32 v238, v240, 6, 6\n"
    "v_lshl_add_u32 v53, v238, 13, v53\n"
    "v_bfe_u32 v238, v240, 7, 5\n"
    "v_lshl_add_u32 v54, v238, 16, v54\n"
    "v_and_b32 v238, 63, v240\n"
    "v_lshl_add_u32 v54, v238, 10, v54\n"
    "s_mov_b64 exec, 0xffff\n"
    ".Lbp90:\n"
    "global_load_dwordx4 v[44:47], v53, s[16:17] sc1\n"
    "s_sub_i32 s48, s48, 1\n"
    "s_cmp_gt_i32 s48, 0\n"
    "s_cselect_b64 s[52:53], -1, 0\n"
    "s_waitcnt vmcnt(0)\n"
    "v_xor_b32 v238, s38, v45\n"
    "v_bitop3_b32 v238, v238, v47, s38 bitop3:0xf6\n"
    "v_cmp_ne_u32 vcc, 0, v238\n"
    "s_and_b64 vcc, vcc, s[52:53]\n"
    "s_cbranch_scc1 .Lbp90\n"
    "s_mov_b64 exec, -1\n"
    "s_mov_b32 s56, 64\n"
    ".Lwu91:\n"
    "s_memrealtime s[54:55]\n"
    "s_sub_u32 s56, s56, 1\n"
    "s_waitcnt lgkmcnt(0)\n"
    "s_sub_u32 s54, s81, s54\n"
    "s_min_i32 s54, s54, s56\n"
    "s_cmp_gt_i32 s54, 0\n"
    "s_cbranch_scc1 .Lwu91\n"
    ".Lyp92:\n"
    "global_load_dwordx2 v[16:17], v52, s[16:17] sc1\n"
    "global_load_dwordx2 v[18:19], v52, s[84:85] sc1\n"
    "global_load_dwordx2 v[20:21], v52, s[86:87] sc1\n"
    "global_load_dwordx2 v[22:23], v52, s[88:89] sc1\n"
    "global_load_dwordx2 v[24:25], v52, s[90:91] sc1\n"
    "global_load_dwordx2 v[26:27], v52, s[92:93] sc1\n"
    "global_load_dwordx2 v[28:29], v52, s[94:95] sc1\n"
    "s_sub_i32 s48, s48, 1\n"
    "s_cmp_gt_i32 s48, 0\n"
    "s_cselect_b64 s[52:53], -1, 0\n"
    "s_waitcnt vmcnt(6)\n"
    "v_xor_b32 v238, s38, v17\n"
    "v_and_b32 v61, s50, v16\n"
    "v_lshlrev_b32 v60, 16, v16\n"
    "s_waitcnt vmcnt(5)\n"
    "v_bitop3_b32 v238, v238, v19, s38 bitop3:0xf6\n"
    "v_and_b32 v19, s50, v18\n"
    "v_lshlrev_b32 v18, 16, v18\n"
    "v_pk_add_f32 v[60:61], v[60:61], v[18:19]\n"
    "s_waitcnt vmcnt(4)\n"
    "v_bitop3_b32 v238, v238, v21, s38 bitop3:0xf6\n"
    "v_and_b32 v21, s50, v20\n"
    "v_lshlrev_b32 v20, 16, v20\n"
    "v_pk_add_f32 v[60:61], v[60:61], v[20:21]\n"
    "s_waitcnt vmcnt(3)\n"
    "v_bitop3_b32 v238, v238, v23, s38 bitop3:0xf6\n"
    "v_and_b32 v23, s50, v22\n"
    "v_lshlrev_b32 v22, 16, v22\n"
    "v_pk_add_f32 v[60:61], v[60:61], v[22:23]\n"
    "s_waitcnt vmcnt(2)\n"
    "v_bitop3_b32 v238, v238, v25, s38 bitop3:0xf6\n"
    "v_and_b32 v25, s50, v24\n"
    "v_lshlrev_b32 v24, 16, v24\n"
    "v_pk_add_f32 v[60:61], v[60:61], v[24:25]\n"
    "s_waitcnt vmcnt(1)\n"
    "v_bitop3_b32 v238, v238, v27, s38 bitop3:0xf6\n"
    "v_and_b32 v27, s50, v26\n"
    "v_lshlrev_b32 v26, 16, v26\n"
    "v_pk_add_f32 v[60:61], v[60:61], v[26:27]\n"
    "s_waitcnt vmcnt(0)\n"
    "v_bitop3_b32 v238, v238, v29, s38 bitop3:0xf6\n"
    "v_and_b32 v29, s50, v28\n"
    "v_lshlrev_b32 v28, 16, v28\n"
    "v_pk_add_f32 v[60:61], v[60:61], v[28:29]\n"
    "v_cmp_ne_u32 vcc, 0, v238\n"
    "s_and_b64 vcc, vcc, s[52:53]\n"
    "s_cbranch_scc1 .Lyp92\n"
    "s_mov_b32 vcc_lo, 0xff00ff00\n"
    "s_mov_b32 vcc_hi, 0xff00ff00\n"
    "v_add_f32_dpp v60, v60, v60 row_ror:8 row_mask:0xf bank_mask:0xf bound_ctrl:1\n"
    "v_add_f32_dpp v61, v61, v61 row_ror:8 row_mask:0xf bank_mask:0xf bound_ctrl:1\n"
    "v_cndmask_b32 v60, v60, v61, vcc\n"
    "v_mov_b32 v61, v60\n"
    "s_nop 1\n"
    "v_permlane16_swap_b32 v60, v61\n"
    "v_add_f32 v60, v60, v61\n"
    "v_mov_b32 v61, v60\n"
    "s_nop 1\n"
    "v_permlane32_swap_b32 v60, v61\n"
    "v_add_f32 v60, v60, v61\n"
    "v_fmac_f32 v44, v46, v60\n"
    "v_cvt_pk_bf16_f32 v44, v44, v44\n"
    "s_mov_b64 exec, 0xffff\n"
    "global_store_short v54, v44, s[22:23]\n"
    "s_mov_b64 exec, -1\n"
    "s_branch .Lexit\n"
    ".Lfk84:\n"
    "s_barrier\n"
    "ds_read_b128 v[56:59], v250\n"
    "s_waitcnt lgkmcnt(0)\n"
    "v_readfirstlane_b32 s76, v56\n"
    "v_readfirstlane_b32 s77, v57\n"
    "v_readfirstlane_b32 s83, v58\n"
    ".Lrx83:\n"
    "s_cmp_eq_u32 s83, s76\n"
    "s_cbranch_scc1 .Lexit\n"
    "s_cmp_eq_u32 s83, s77\n"
    "s_cbranch_scc1 .Lexit\n"
    "s_lshl_b32 s58, s80, 13\n"
    "s_add_u32 s58, s58, 1380352\n"
    "v_add_u32 v242, s58, v2\n"
    "s_and_b32 s57, s83, 63\n"
    "s_lshr_b32 s56, s80, 1\n"
    "s_lshl_b32 s56, s56, 6\n"
    "s_add_u32 s57, s57, s56\n"
    "s_lshl_b32 s57, s57, 10\n"
    "v_lshrrev_b32 v239, 3, v1\n"
    "v_lshl_add_u32 v238, v239, 1, v13\n"
    "v_add_u32 v243, s57, v238\n"
    ".Lrw82:\n"
    "global_load_dwordx2 v[88:89], v242, s[16:17] offset:-4096 sc1\n"
    "global_load_dwordx2 v[90:91], v242, s[16:17] offset:-3072 sc1\n"
    "global_load_dwordx2 v[92:93], v242, s[16:17] offset:-2048 sc1\n"
    "global_load_dwordx2 v[94:95], v242, s[16:17] offset:-1024 sc1\n"
    "global_load_dwordx2 v[96:97], v242, s[16:17] offset:0 sc1\n"
    "global_load_dwordx2 v[98:99], v242, s[16:17] offset:1024 sc1\n"
    "global_load_dwordx2 v[100:101], v242, s[16:17] offset:2048 sc1\n"
    "global_load_dwordx2 v[102:103], v242, s[16:17] offset:3072 sc1\n"
    "s_sub_i32 s48, s48, 1\n"
    "s_cmp_gt_i32 s48, 0\n"
    "s_cselect_b64 s[52:53], -1, 0\n"
    "s_waitcnt vmcnt(0)\n"
    "v_xor_b32 v238, s38, v89\n"
    "v_bitop3_b32 v238, v238, v91, s38 bitop3:0xf6\n"
    "v_bitop3_b32 v238, v238, v93, s38 bitop3:0xf6\n"
    "v_bitop3_b32 v238, v238, v95, s38 bitop3:0xf6\n"
    "v_bitop3_b32 v238, v238, v97, s38 bitop3:0xf6\n"
    "v_bitop3_b32 v238, v238, v99, s38 bitop3:0xf6\n"
    "v_bitop3_b32 v238, v238, v101, s38 bitop3:0xf6\n"
    "v_bitop3_b32 v238, v238, v103, s38 bitop3:0xf6\n"
    "v_cmp_ne_u32 vcc, 0, v238\n"
    "s_and_b64 vcc, vcc, s[52:53]\n"
    "s_cbranch_scc1 .Lrw82\n"
    "v_cvt_pk_bf16_f32 v88, v88, v88\n"
    "global_store_short v243, v88, s[22:23] offset:0 sc1\n"
    "v_cvt_pk_bf16_f32 v90, v90, v90\n"
    "global_store_short v243, v90, s[22:23] offset:128 sc1\n"
    "v_cvt_pk_bf16_f32 v92, v92, v92\n"
    "global_store_short v243, v92, s[22:23] offset:256 sc1\n"
    "v_cvt_pk_bf16_f32 v94, v94, v94\n"
    "global_store_short v243, v94, s[22:23] offset:384 sc1\n"
    "v_cvt_pk_bf16_f32 v96, v96, v96\n"
    "global_store_short v243, v96, s[22:23] offset:512 sc1\n"
    "v_cvt_pk_bf16_f32 v98, v98, v98\n"
    "global_store_short v243, v98, s[22:23] offset:640 sc1\n"
    "v_cvt_pk_bf16_f32 v100, v100, v100\n"
    "global_store_short v243, v100, s[22:23] offset:768 sc1\n"
    "v_cvt_pk_bf16_f32 v102, v102, v102\n"
    "global_store_short v243, v102, s[22:23] offset:896 sc1\n"
    ".Lexit:\n"
    "s_endpgm\n"
    ".Lmr1:\n"
    "global_load_dwordx4 v[208:211], v230, s[16:17] sc1\n"
    "v_add_u32 v231, 24576, v230\n"
    "global_load_dwordx4 v[212:215], v231, s[16:17] sc1\n"
    "v_add_u32 v230, 24576, v231\n"
    "global_load_dwordx4 v[216:219], v230, s[16:17] sc1\n"
    "v_add_u32 v231, 24576, v230\n"
    "global_load_dwordx4 v[220:223], v231, s[16:17] sc1\n"
    "v_add_u32 v230, -73728, v231\n"
    "s_sub_i32 s48, s48, 1\n"
    "s_cmp_gt_i32 s48, 0\n"
    "s_cselect_b64 s[52:53], -1, 0\n"
    "s_waitcnt vmcnt(0)\n"
    "v_xor_b32 v239, s38, v209\n"
    "v_bitop3_b32 v239, v239, v211, s38 bitop3:0xf6\n"
    "v_bitop3_b32 v239, v239, v213, s38 bitop3:0xf6\n"
    "v_bitop3_b32 v239, v239, v215, s38 bitop3:0xf6\n"
    "v_bitop3_b32 v239, v239, v217, s38 bitop3:0xf6\n"
    "v_bitop3_b32 v239, v239, v219, s38 bitop3:0xf6\n"
    "v_bitop3_b32 v239, v239, v221, s38 bitop3:0xf6\n"
    "v_bitop3_b32 v239, v239, v223, s38 bitop3:0xf6\n"
    "v_cmp_ne_u32 vcc, 0, v239\n"
    "s_and_b64 vcc, vcc, s[52:53]\n"
    "s_cbranch_scc1 .Lmr1\n"
    "s_branch .Lmb2\n"
    ".Lkr3:\n"
    ".Ltw7:\n"
    "s_mov_b32 exec_lo, 0xf000f\n"
    "s_mov_b32 exec_hi, 0xf000f\n"
    "global_load_dwordx2 v[86:87], v15, s[16:17] sc1\n"
    "s_mov_b64 exec, -1\n"
    "s_sub_i32 s48, s48, 1\n"
    "s_cmp_gt_i32 s48, 0\n"
    "s_cselect_b64 s[52:53], -1, 0\n"
    "s_waitcnt vmcnt(0)\n"
    "v_cmp_ne_u32 vcc, s38, v87\n"
    "s_and_b64 vcc, vcc, s[52:53]\n"
    "s_cbranch_scc1 .Ltw7\n"
    "s_sub_u32 s61, s3, 526336\n"
    "s_max_i32 s61, s61, s4\n"
    "s_add_u32 s62, s16, s61\n"
    "s_addc_u32 s63, s17, 0\n"
    "global_load_dwordx4 v[70:73], v2, s[62:63] sc1\n"
    "s_sub_u32 s61, s3, 394752\n"
    "s_max_i32 s61, s61, s4\n"
    "s_add_u32 s62, s16, s61\n"
    "s_addc_u32 s63, s17, 0\n"
    "global_load_dwordx4 v[74:77], v2, s[62:63] sc1\n"
    "s_sub_u32 s61, s3, 328960\n"
    "s_max_i32 s61, s61, s4\n"
    "s_add_u32 s62, s16, s61\n"
    "s_addc_u32 s63, s17, 0\n"
    "global_load_dwordx4 v[78:81], v2, s[62:63] sc1\n"
    "s_sub_u32 s61, s3, 263168\n"
    "s_max_i32 s61, s61, s4\n"
    "s_add_u32 s62, s16, s61\n"
    "s_addc_u32 s63, s17, 0\n"
    "global_load_dwordx4 v[82:85], v2, s[62:63] sc1\n"
    "s_waitcnt vmcnt(0)\n"
    "v_add3_u32 v238, v70, v71, v72\n"
    "v_add3_u32 v238, v238, v73, v74\n"
    "v_add3_u32 v238, v238, v75, v76\n"
    "v_add3_u32 v238, v238, v77, v78\n"
    "v_add3_u32 v238, v238, v79, v80\n"
    "v_add3_u32 v238, v238, v81, v82\n"
    "v_add3_u32 v238, v238, v83, v84\n"
    "v_add_u32 v238, v238, v85\n"
    "v_sub_u32 v238, v238, v86\n"
    "s_nop 1\n"
    "v_add_u32_dpp v238, v238, v238 row_ror:8 row_mask:0xf bank_mask:0xf bound_ctrl:1\n"
    "s_nop 1\n"
    "v_add_u32_dpp v238, v238, v238 row_ror:4 row_mask:0xf bank_mask:0xf bound_ctrl:1\n"
    "s_nop 1\n"
    "v_add_u32_dpp v238, v238, v238 row_ror:2 row_mask:0xf bank_mask:0xf bound_ctrl:1\n"
    "s_nop 1\n"
    "v_add_u32_dpp v238, v238, v238 row_ror:1 row_mask:0xf bank_mask:0xf bound_ctrl:1\n"
    "v_bitop3_b32 v238, v238, v87, s38 bitop3:0xf6\n"
    "v_cmp_ne_u32 vcc, 0, v238\n"
    "s_and_b64 vcc, vcc, s[52:53]\n"
    "s_cbranch_scc1 .Lkr3\n"
    "s_branch .Lkb4\n"
    ".Lkf5:\n"
    "s_bitcmp1_b32 s51, 0\n"
    "s_cbranch_scc0 .Lks8\n"
    "v_mov_b32 v66, 0\n"
    "v_mov_b32 v67, 0\n"
    "v_mov_b32 v68, 0\n"
    "v_mov_b32 v69, 0\n"
    ".Lks8:\n"
    "s_bitcmp1_b32 s51, 1\n"
    "s_cbranch_scc0 .Lks9\n"
    "v_mov_b32 v70, 0\n"
    "v_mov_b32 v71, 0\n"
    "v_mov_b32 v72, 0\n"
    "v_mov_b32 v73, 0\n"
    ".Lks9:\n"
    "s_bitcmp1_b32 s51, 2\n"
    "s_cbranch_scc0 .Lks10\n"
    "v_mov_b32 v74, 0\n"
    "v_mov_b32 v75, 0\n"
    "v_mov_b32 v76, 0\n"
    "v_mov_b32 v77, 0\n"
    ".Lks10:\n"
    "s_bitcmp1_b32 s51, 3\n"
    "s_cbranch_scc0 .Lks11\n"
    "v_mov_b32 v78, 0\n"
    "v_mov_b32 v79, 0\n"
    "v_mov_b32 v80, 0\n"
    "v_mov_b32 v81, 0\n"
    ".Lks11:\n"
    "s_bitcmp1_b32 s51, 4\n"
    "s_cbranch_scc0 .Lks12\n"
    "v_mov_b32 v82, 0\n"
    "v_mov_b32 v83, 0\n"
    "v_mov_b32 v84, 0\n"
    "v_mov_b32 v85, 0\n"
    ".Lks12:\n"
    "s_branch .Lkfb6\n"
    ".Lmr13:\n"
    "global_load_dwordx4 v[208:211], v230, s[16:17] sc1\n"
    "v_add_u32 v231, 24576, v230\n"
    "global_load_dwordx4 v[212:215], v231, s[16:17] sc1\n"
    "v_add_u32 v230, 24576, v231\n"
    "global_load_dwordx4 v[216:219], v230, s[16:17] sc1\n"
    "v_add_u32 v231, 24576, v230\n"
    "global_load_dwordx4 v[220:223], v231, s[16:17] sc1\n"
    "v_add_u32 v230, -73728, v231\n"
    "s_sub_i32 s48, s48, 1\n"
    "s_cmp_gt_i32 s48, 0\n"
    "s_cselect_b64 s[52:53], -1, 0\n"
    "s_waitcnt vmcnt(0)\n"
    "v_xor_b32 v239, s38, v209\n"
    "v_bitop3_b32 v239, v239, v211, s38 bitop3:0xf6\n"
    "v_bitop3_b32 v239, v239, v213, s38 bitop3:0xf6\n"
    "v_bitop3_b32 v239, v239, v215, s38 bitop3:0xf6\n"
    "v_bitop3_b32 v239, v239, v217, s38 bitop3:0xf6\n"
    "v_bitop3_b32 v239, v239, v219, s38 bitop3:0xf6\n"
    "v_bitop3_b32 v239, v239, v221, s38 bitop3:0xf6\n"
    "v_bitop3_b32 v239, v239, v223, s38 bitop3:0xf6\n"
    "v_cmp_ne_u32 vcc, 0, v239\n"
    "s_and_b64 vcc, vcc, s[52:53]\n"
    "s_cbranch_scc1 .Lmr13\n"
    "s_branch .Lmb14\n"
    ".Lkr15:\n"
    ".Ltw19:\n"
    "s_mov_b32 exec_lo, 0xf000f\n"
    "s_mov_b32 exec_hi, 0xf000f\n"
    "global_load_dwordx2 v[86:87], v15, s[16:17] sc1\n"
    "s_mov_b64 exec, -1\n"
    "s_sub_i32 s48, s48, 1\n"
    "s_cmp_gt_i32 s48, 0\n"
    "s_cselect_b64 s[52:53], -1, 0\n"
    "s_waitcnt vmcnt(0)\n"
    "v_cmp_ne_u32 vcc, s38, v87\n"
    "s_and_b64 vcc, vcc, s[52:53]\n"
    "s_cbranch_scc1 .Ltw19\n"
    "s_sub_u32 s61, s3, 460544\n"
    "s_max_i32 s61, s61, s4\n"
    "s_add_u32 s62, s16, s61\n"
    "s_addc_u32 s63, s17, 0\n"
    "global_load_dwordx4 v[66:69], v2, s[62:63] sc1\n"
    "s_sub_u32 s61, s3, 197376\n"
    "s_max_i32 s61, s61, s4\n"
    "s_add_u32 s62, s16, s61\n"
    "s_addc_u32 s63, s17, 0\n"
    "global_load_dwordx4 v[74:77], v2, s[62:63] sc1\n"
    "s_sub_u32 s61, s3, 131584\n"
    "s_max_i32 s61, s61, s4\n"
    "s_add_u32 s62, s16, s61\n"
    "s_addc_u32 s63, s17, 0\n"
    "global_load_dwordx4 v[78:81], v2, s[62:63] sc1\n"
    "s_sub_u32 s61, s3, 65792\n"
    "s_max_i32 s61, s61, s4\n"
    "s_add_u32 s62, s16, s61\n"
    "s_addc_u32 s63, s17, 0\n"
    "global_load_dwordx4 v[82:85], v2, s[62:63] sc1\n"
    "s_waitcnt vmcnt(0)\n"
    "v_add3_u32 v238, v66, v67, v68\n"
    "v_add3_u32 v238, v238, v69, v74\n"
    "v_add3_u32 v238, v238, v75, v76\n"
    "v_add3_u32 v238, v238, v77, v78\n"
    "v_add3_u32 v238, v238, v79, v80\n"
    "v_add3_u32 v238, v238, v81, v82\n"
    "v_add3_u32 v238, v238, v83, v84\n"
    "v_add_u32 v238, v238, v85\n"
    "v_sub_u32 v238, v238, v86\n"
    "s_nop 1\n"
    "v_add_u32_dpp v238, v238, v238 row_ror:8 row_mask:0xf bank_mask:0xf bound_ctrl:1\n"
    "s_nop 1\n"
    "v_add_u32_dpp v238, v238, v238 row_ror:4 row_mask:0xf bank_mask:0xf bound_ctrl:1\n"
    "s_nop 1\n"
    "v_add_u32_dpp v238, v238, v238 row_ror:2 row_mask:0xf bank_mask:0xf bound_ctrl:1\n"
    "s_nop 1\n"
    "v_add_u32_dpp v238, v238, v238 row_ror:1 row_mask:0xf bank_mask:0xf bound_ctrl:1\n"
    "v_bitop3_b32 v238, v238, v87, s38 bitop3:0xf6\n"
    "v_cmp_ne_u32 vcc, 0, v238\n"
    "s_and_b64 vcc, vcc, s[52:53]\n"
    "s_cbranch_scc1 .Lkr15\n"
    "s_branch .Lkb16\n"
    ".Lkf17:\n"
    "s_bitcmp1_b32 s51, 0\n"
    "s_cbranch_scc0 .Lks20\n"
    "v_mov_b32 v66, 0\n"
    "v_mov_b32 v67, 0\n"
    "v_mov_b32 v68, 0\n"
    "v_mov_b32 v69, 0\n"
    ".Lks20:\n"
    "s_bitcmp1_b32 s51, 1\n"
    "s_cbranch_scc0 .Lks21\n"
    "v_mov_b32 v70, 0\n"
    "v_mov_b32 v71, 0\n"
    "v_mov_b32 v72, 0\n"
    "v_mov_b32 v73, 0\n"
    ".Lks21:\n"
    "s_bitcmp1_b32 s51, 2\n"
    "s_cbranch_scc0 .Lks22\n"
    "v_mov_b32 v74, 0\n"
    "v_mov_b32 v75, 0\n"
    "v_mov_b32 v76, 0\n"
    "v_mov_b32 v77, 0\n"
    ".Lks22:\n"
    "s_bitcmp1_b32 s51, 3\n"
    "s_cbranch_scc0 .Lks23\n"
    "v_mov_b32 v78, 0\n"
    "v_mov_b32 v79, 0\n"
    "v_mov_b32 v80, 0\n"
    "v_mov_b32 v81, 0\n"
    ".Lks23:\n"
    "s_bitcmp1_b32 s51, 4\n"
    "s_cbranch_scc0 .Lks24\n"
    "v_mov_b32 v82, 0\n"
    "v_mov_b32 v83, 0\n"
    "v_mov_b32 v84, 0\n"
    "v_mov_b32 v85, 0\n"
    ".Lks24:\n"
    "s_branch .Lkfb18\n"
    ".Lvf28:\n"
    "s_bitcmp1_b32 s51, 0\n"
    "s_cbranch_scc0 .Lvs30\n"
    "v_mov_b32 v44, 0\n"
    "v_mov_b32 v45, 0\n"
    "v_mov_b32 v46, 0\n"
    "v_mov_b32 v47, 0\n"
    "v_mov_b32 v16, 0\n"
    "v_mov_b32 v17, 0\n"
    "v_mov_b32 v18, 0\n"
    "v_mov_b32 v19, 0\n"
    ".Lvs30:\n"
    "s_bitcmp1_b32 s51, 1\n"
    "s_cbranch_scc0 .Lvs31\n"
    "v_mov_b32 v48, 0\n"
    "v_mov_b32 v49, 0\n"
    "v_mov_b32 v50, 0\n"
    "v_mov_b32 v51, 0\n"
    "v_mov_b32 v20, 0\n"
    "v_mov_b32 v21, 0\n"
    "v_mov_b32 v22, 0\n"
    "v_mov_b32 v23, 0\n"
    ".Lvs31:\n"
    "s_bitcmp1_b32 s51, 2\n"
    "s_cbranch_scc0 .Lvs32\n"
    "v_mov_b32 v52, 0\n"
    "v_mov_b32 v53, 0\n"
    "v_mov_b32 v54, 0\n"
    "v_mov_b32 v55, 0\n"
    "v_mov_b32 v24, 0\n"
    "v_mov_b32 v25, 0\n"
    "v_mov_b32 v26, 0\n"
    "v_mov_b32 v27, 0\n"
    ".Lvs32:\n"
    "s_bitcmp1_b32 s51, 3\n"
    "s_cbranch_scc0 .Lvs33\n"
    "v_mov_b32 v56, 0\n"
    "v_mov_b32 v57, 0\n"
    "v_mov_b32 v58, 0\n"
    "v_mov_b32 v59, 0\n"
    "v_mov_b32 v28, 0\n"
    "v_mov_b32 v29, 0\n"
    "v_mov_b32 v30, 0\n"
    "v_mov_b32 v31, 0\n"
    ".Lvs33:\n"
    "s_bitcmp1_b32 s51, 4\n"
    "s_cbranch_scc0 .Lvs34\n"
    "v_mov_b32 v60, 0\n"
    "v_mov_b32 v61, 0\n"
    "v_mov_b32 v62, 0\n"
    "v_mov_b32 v63, 0\n"
    "v_mov_b32 v32, 0\n"
    "v_mov_b32 v33, 0\n"
    "v_mov_b32 v34, 0\n"
    "v_mov_b32 v35, 0\n"
    ".Lvs34:\n"
    "s_branch .Lvfb29\n"
    ".Lvr26:\n"
    ".Ltw36:\n"
    "s_mov_b32 exec_lo, 0xf000f\n"
    "s_mov_b32 exec_hi, 0xf000f\n"
    "global_load_dwordx2 v[64:65], v15, s[16:17] offset:128 sc1\n"
    "s_mov_b64 exec, -1\n"
    "s_sub_i32 s48, s48, 1\n"
    "s_cmp_gt_i32 s48, 0\n"
    "s_cselect_b64 s[52:53], -1, 0\n"
    "s_waitcnt vmcnt(0)\n"
    "v_cmp_ne_u32 vcc, s38, v65\n"
    "s_and_b64 vcc, vcc, s[52:53]\n"
    "s_cbranch_scc1 .Ltw36\n"
    "s_cmp_ge_u32 s30, 4\n"
    "s_cbranch_scc1 .Lvy35\n"
    "s_add_u32 m0, s1, 2048\n"
    "s_sub_u32 s61, s3, 526336\n"
    "s_max_i32 s61, s61, s4\n"
    "s_add_u32 s62, s16, s61\n"
    "s_addc_u32 s63, s17, 0\n"
    "global_load_lds_dwordx4 v2, s[62:63] offset:1024 sc1\n"
    "s_add_u32 m0, s1, 3072\n"
    "s_sub_u32 s61, s3, 394752\n"
    "s_max_i32 s61, s61, s4\n"
    "s_add_u32 s62, s16, s61\n"
    "s_addc_u32 s63, s17, 0\n"
    "global_load_lds_dwordx4 v2, s[62:63] offset:1024 sc1\n"
    "s_add_u32 m0, s1, 4096\n"
    "s_sub_u32 s61, s3, 328960\n"
    "s_max_i32 s61, s61, s4\n"
    "s_add_u32 s62, s16, s61\n"
    "s_addc_u32 s63, s17, 0\n"
    "global_load_lds_dwordx4 v2, s[62:63] offset:1024 sc1\n"
    "s_add_u32 m0, s1, 5120\n"
    "s_sub_u32 s61, s3, 263168\n"
    "s_max_i32 s61, s61, s4\n"
    "s_add_u32 s62, s16, s61\n"
    "s_addc_u32 s63, s17, 0\n"
    "global_load_lds_dwordx4 v2, s[62:63] offset:1024 sc1\n"
    "s_waitcnt vmcnt(0)\n"
    "s_branch .Lvb27\n"
    ".Lvy35:\n"
    "s_add_u32 m0, s1, 2048\n"
    "s_sub_u32 s61, s3, 460544\n"
    "s_max_i32 s61, s61, s4\n"
    "s_add_u32 s62, s16, s61\n"
    "s_addc_u32 s63, s17, 0\n"
    "global_load_lds_dwordx4 v2, s[62:63] offset:1024 sc1\n"
    "s_add_u32 m0, s1, 3072\n"
    "s_sub_u32 s61, s3, 197376\n"
    "s_max_i32 s61, s61, s4\n"
    "s_add_u32 s62, s16, s61\n"
    "s_addc_u32 s63, s17, 0\n"
    "global_load_lds_dwordx4 v2, s[62:63] offset:1024 sc1\n"
    "s_add_u32 m0, s1, 4096\n"
    "s_sub_u32 s61, s3, 131584\n"
    "s_max_i32 s61, s61, s4\n"
    "s_add_u32 s62, s16, s61\n"
    "s_addc_u32 s63, s17, 0\n"
    "global_load_lds_dwordx4 v2, s[62:63] offset:1024 sc1\n"
    "s_add_u32 m0, s1, 5120\n"
    "s_sub_u32 s61, s3, 65792\n"
    "s_max_i32 s61, s61, s4\n"
    "s_add_u32 s62, s16, s61\n"
    "s_addc_u32 s63, s17, 0\n"
    "global_load_lds_dwordx4 v2, s[62:63] offset:1024 sc1\n"
    "s_waitcnt vmcnt(0)\n"
    "s_branch .Lvb27\n"
    ".Lh3:\n"
    "s_lshl3_add_u32 s54, s33, s34\n"
    "s_lshl_b32 s54, s54, 7\n"
    "s_add_u32 s54, s54, 1114112\n"
    "v_mov_b32 v201, s38\n"
    "v_lshlrev_b32 v198, 2, v10\n"
    "s_add_u32 s62, s16, s54\n"
    "s_addc_u32 s63, s17, 0\n"
    "s_waitcnt vmcnt(0) lgkmcnt(0)\n"
    "s_barrier\n"
    "s_memrealtime s[10:11]\n"
    "s_waitcnt lgkmcnt(0)\n"
    "v_mov_b32 v200, s10\n"
    "s_mov_b64 exec, 0xffff\n"
    "global_store_dwordx2 v198, v[200:201], s[62:63] sc1\n"
    "s_mov_b64 exec, -1\n"
    "s_branch .Lh3b\n"
    ".Lex37:\n"
    "s_lshl2_add_u32 s56, s34, s39\n"
    "s_lshl_b32 s56, s56, 11\n"
    "s_add_u32 s56, s56, 2621440\n"
    "v_mov_b32 v242, 0xff800000\n"
    "v_mov_b32 v246, 0xff800000\n"
    "s_mov_b32 s57, 0\n"
    "v_mov_b32 v150, 0\n"
    "v_mov_b32 v151, 0\n"
    "v_mov_b32 v152, 0\n"
    "v_mov_b32 v153, 0\n"
    "v_mov_b32 v154, 0\n"
    "v_mov_b32 v155, 0\n"
    "v_mov_b32 v156, 0\n"
    "v_mov_b32 v157, 0\n"
    "v_mov_b32 v158, 0\n"
    "v_mov_b32 v159, 0\n"
    "v_mov_b32 v160, 0\n"
    "v_mov_b32 v161, 0\n"
    "v_mov_b32 v162, 0\n"
    "v_mov_b32 v163, 0\n"
    "v_mov_b32 v164, 0\n"
    "v_mov_b32 v165, 0\n"
    ".Lx39:\n"
    "s_mov_b32 s54, 0\n"
    ".Lx40:\n"
    "s_lshr_b32 s55, s30, 2\n"
    "s_lshl1_add_u32 s55, s33, s55\n"
    "s_sub_i32 s55, s55, s54\n"
    "s_cmp_lt_i32 s55, 0\n"
    "s_cbranch_scc1 .Lx42\n"
    "s_mul_i32 s55, s55, 65792\n"
    "s_add_u32 s55, s55, s56\n"
    "v_add_u32 v245, s55, v2\n"
    "global_load_dwordx4 v[66:69], v245, s[16:17] sc1\n"
    "global_load_dwordx4 v[44:47], v245, s[16:17] offset:1024 sc1\n"
    "v_and_b32 v243, 31, v1\n"
    "v_lshrrev_b32 v244, 5, v1\n"
    "v_lshlrev_b32 v244, 2, v244\n"
    "v_sub_u32 v243, v243, v244\n"
    "v_lshl_add_u32 v243, s54, 5, v243\n"
    "s_waitcnt vmcnt(0)\n"
    "v_mfma_f32_32x32x16_bf16 v[86:101], v[66:69], v[190:193], 0\n"
    "v_mov_b32 v16, s49\n"
    "v_mov_b32 v17, s49\n"
    "v_mov_b32 v18, s49\n"
    "v_mov_b32 v19, s49\n"
    "v_permlane16_swap_b32 v44, v16\n"
    "v_permlane16_swap_b32 v45, v17\n"
    "v_permlane16_swap_b32 v46, v18\n"
    "v_permlane16_swap_b32 v47, v19\n"
    "v_subrev_u32 v244, 0, v243\n"
    "v_cmp_gt_u32 vcc, 256, v244\n"
    "s_nop 1\n"
    "v_cndmask_b32 v86, v246, v86, vcc\n"
    "v_subrev_u32 v244, 1, v243\n"
    "v_cmp_gt_u32 vcc, 256, v244\n"
    "s_nop 1\n"
    "v_cndmask_b32 v87, v246, v87, vcc\n"
    "v_subrev_u32 v244, 2, v243\n"
    "v_cmp_gt_u32 vcc, 256, v244\n"
    "s_nop 1\n"
    "v_cndmask_b32 v88, v246, v88, vcc\n"
    "v_subrev_u32 v244, 3, v243\n"
    "v_cmp_gt_u32 vcc, 256, v244\n"
    "s_nop 1\n"
    "v_cndmask_b32 v89, v246, v89, vcc\n"
    "v_subrev_u32 v244, 8, v243\n"
    "v_cmp_gt_u32 vcc, 256, v244\n"
    "s_nop 1\n"
    "v_cndmask_b32 v90, v246, v90, vcc\n"
    "v_subrev_u32 v244, 9, v243\n"
    "v_cmp_gt_u32 vcc, 256, v244\n"
    "s_nop 1\n"
    "v_cndmask_b32 v91, v246, v91, vcc\n"
    "v_subrev_u32 v244, 10, v243\n"
    "v_cmp_gt_u32 vcc, 256, v244\n"
    "s_nop 1\n"
    "v_cndmask_b32 v92, v246, v92, vcc\n"
    "v_subrev_u32 v244, 11, v243\n"
    "v_cmp_gt_u32 vcc, 256, v244\n"
    "s_nop 1\n"
    "v_cndmask_b32 v93, v246, v93, vcc\n"
    "v_subrev_u32 v244, 16, v243\n"
    "v_cmp_gt_u32 vcc, 256, v244\n"
    "s_nop 1\n"
    "v_cndmask_b32 v94, v246, v94, vcc\n"
    "v_subrev_u32 v244, 17, v243\n"
    "v_cmp_gt_u32 vcc, 256, v244\n"
    "s_nop 1\n"
    "v_cndmask_b32 v95, v246, v95, vcc\n"
    "v_subrev_u32 v244, 18, v243\n"
    "v_cmp_gt_u32 vcc, 256, v244\n"
    "s_nop 1\n"
    "v_cndmask_b32 v96, v246, v96, vcc\n"
    "v_subrev_u32 v244, 19, v243\n"
    "v_cmp_gt_u32 vcc, 256, v244\n"
    "s_nop 1\n"
    "v_cndmask_b32 v97, v246, v97, vcc\n"
    "v_subrev_u32 v244, 24, v243\n"
    "v_cmp_gt_u32 vcc, 256, v244\n"
    "s_nop 1\n"
    "v_cndmask_b32 v98, v246, v98, vcc\n"
    "v_subrev_u32 v244, 25, v243\n"
    "v_cmp_gt_u32 vcc, 256, v244\n"
    "s_nop 1\n"
    "v_cndmask_b32 v99, v246, v99, vcc\n"
    "v_subrev_u32 v244, 26, v243\n"
    "v_cmp_gt_u32 vcc, 256, v244\n"
    "s_nop 1\n"
    "v_cndmask_b32 v100, v246, v100, vcc\n"
    "v_subrev_u32 v244, 27, v243\n"
    "v_cmp_gt_u32 vcc, 256, v244\n"
    "s_nop 1\n"
    "v_cndmask_b32 v101, v246, v101, vcc\n"
    "s_cmp_lg_u32 s57, 0\n"
    "s_cbranch_scc1 .Lx41\n"
    "v_max3_f32 v242, v242, v86, v87\n"
    "v_max3_f32 v242, v242, v88, v89\n"
    "v_max3_f32 v242, v242, v90, v91\n"
    "v_max3_f32 v242, v242, v92, v93\n"
    "v_max3_f32 v242, v242, v94, v95\n"
    "v_max3_f32 v242, v242, v96, v97\n"
    "v_max3_f32 v242, v242, v98, v99\n"
    "v_max3_f32 v242, v242, v100, v101\n"
    "s_branch .Lx43\n"
    ".Lx41:\n"
    "v_sub_f32 v86, v86, v242\n"
    "v_sub_f32 v87, v87, v242\n"
    "v_sub_f32 v88, v88, v242\n"
    "v_sub_f32 v89, v89, v242\n"
    "v_sub_f32 v90, v90, v242\n"
    "v_sub_f32 v91, v91, v242\n"
    "v_sub_f32 v92, v92, v242\n"
    "v_sub_f32 v93, v93, v242\n"
    "v_sub_f32 v94, v94, v242\n"
    "v_sub_f32 v95, v95, v242\n"
    "v_sub_f32 v96, v96, v242\n"
    "v_sub_f32 v97, v97, v242\n"
    "v_sub_f32 v98, v98, v242\n"
    "v_sub_f32 v99, v99, v242\n"
    "v_sub_f32 v100, v100, v242\n"
    "v_sub_f32 v101, v101, v242\n"
    "v_exp_f32 v86, v86\n"
    "v_exp_f32 v87, v87\n"
    "v_exp_f32 v88, v88\n"
    "v_exp_f32 v89, v89\n"
    "v_exp_f32 v90, v90\n"
    "v_exp_f32 v91, v91\n"
    "v_exp_f32 v92, v92\n"
    "v_exp_f32 v93, v93\n"
    "v_exp_f32 v94, v94\n"
    "v_exp_f32 v95, v95\n"
    "v_exp_f32 v96, v96\n"
    "v_exp_f32 v97, v97\n"
    "v_exp_f32 v98, v98\n"
    "v_exp_f32 v99, v99\n"
    "v_exp_f32 v100, v100\n"
    "v_exp_f32 v101, v101\n"
    "v_cvt_pk_bf16_f32 v86, v86, v87\n"
    "v_cvt_pk_bf16_f32 v87, v88, v89\n"
    "v_cvt_pk_bf16_f32 v88, v90, v91\n"
    "v_cvt_pk_bf16_f32 v89, v92, v93\n"
    "v_cvt_pk_bf16_f32 v90, v94, v95\n"
    "v_cvt_pk_bf16_f32 v91, v96, v97\n"
    "v_cvt_pk_bf16_f32 v92, v98, v99\n"
    "v_cvt_pk_bf16_f32 v93, v100, v101\n"
    "v_mfma_f32_32x32x16_bf16 v[150:165], v[44:47], v[86:89], v[150:165]\n"
    "s_nop 0\n"
    "v_mfma_f32_32x32x16_bf16 v[150:165], v[16:19], v[90:93], v[150:165]\n"
    "s_nop 11\n"
    ".Lx43:\n"
    "s_add_u32 s54, s54, 1\n"
    "s_cmp_lt_u32 s54, 9\n"
    "s_cbranch_scc1 .Lx40\n"
    ".Lx42:\n"
    "v_mov_b32 v244, v242\n"
    "s_nop 1\n"
    "v_permlane32_swap_b32 v242, v244\n"
    "v_max_f32 v242, v242, v244\n"
    "s_add_u32 s57, s57, 1\n"
    "s_cmp_lt_u32 s57, 2\n"
    "s_cbranch_scc1 .Lx39\n"
    "v_mov_b32 v166, 0\n"
    "v_mov_b32 v167, 0\n"
    "v_mov_b32 v168, 0\n"
    "v_mov_b32 v169, 0\n"
    "v_mov_b32 v170, 0\n"
    "v_mov_b32 v171, 0\n"
    "v_mov_b32 v172, 0\n"
    "v_mov_b32 v173, 0\n"
    "v_mov_b32 v174, v158\n"
    "s_branch .Lexb38\n"
    ".Lgr44:\n"
    "s_add_u32 s62, s16, s15\n"
    "s_addc_u32 s63, s17, 0\n"
    "global_load_dwordx4 v[212:215], v14, s[62:63] sc1\n"
    "s_sub_i32 s48, s48, 1\n"
    "s_cmp_gt_i32 s48, 0\n"
    "s_cselect_b64 s[52:53], -1, 0\n"
    "s_waitcnt vmcnt(0)\n"
    "v_xor_b32 v238, s38, v213\n"
    "v_bitop3_b32 v238, v238, v215, s38 bitop3:0xf6\n"
    "v_cmp_ne_u32 vcc, 0, v238\n"
    "s_and_b64 vcc, vcc, s[52:53]\n"
    "s_cbranch_scc1 .Lgr44\n"
    "s_branch .Lgb45\n"
    ".Lhr47:\n"
    "v_lshl_add_u32 v198, v1, 7, s58\n"
    "s_mov_b64 exec, 0xff\n"
    ".Lhl49:\n"
    "global_load_dwordx2 v[200:201], v198, s[16:17] sc1\n"
    "s_sub_i32 s48, s48, 1\n"
    "s_cmp_gt_i32 s48, 0\n"
    "s_cselect_b64 s[52:53], -1, 0\n"
    "s_waitcnt vmcnt(0)\n"
    "v_cmp_ne_u32 vcc, s38, v201\n"
    "s_and_b64 vcc, vcc, s[52:53]\n"
    "s_cbranch_scc1 .Lhl49\n"
    "s_mov_b64 exec, -1\n"
    "v_subrev_u32 v200, s10, v200\n"
    "s_nop 1\n"
    "v_max_i32_dpp v200, v200, v200 quad_perm:[1,0,3,2] row_mask:0xf bank_mask:0xf bound_ctrl:1\n"
    "s_nop 1\n"
    "v_max_i32_dpp v200, v200, v200 quad_perm:[2,3,0,1] row_mask:0xf bank_mask:0xf bound_ctrl:1\n"
    "s_nop 1\n"
    "v_max_i32_dpp v200, v200, v200 row_half_mirror row_mask:0xf bank_mask:0xf bound_ctrl:1\n"
    "s_nop 0\n"
    "v_readlane_b32 s70, v200, 0\n"
    "s_max_i32 s70, s70, 0\n"
    "s_min_i32 s70, s70, 250\n"
    "s_branch .Lhb48\n"
    ".Lw3:\n"
    "s_lshl3_add_u32 s54, s36, s37\n"
    "s_lshl_b32 s54, s54, 7\n"
    "s_add_u32 s54, s54, 1179648\n"
    "v_mov_b32 v201, s38\n"
    "v_lshlrev_b32 v198, 2, v10\n"
    "s_add_u32 s62, s16, s54\n"
    "s_addc_u32 s63, s17, 0\n"
    "s_waitcnt lgkmcnt(0)\n"
    "s_barrier\n"
    "s_memrealtime s[10:11]\n"
    "s_waitcnt lgkmcnt(0)\n"
    "v_mov_b32 v200, s10\n"
    "s_mov_b64 exec, 0xffff\n"
    "global_store_dwordx2 v198, v[200:201], s[62:63] sc1\n"
    "s_mov_b64 exec, -1\n"
    "s_branch .Lw3b\n"
    ".Lor54:\n"
    ".Ltw55:\n"
    "s_mov_b32 exec_lo, 0xff00ff\n"
    "s_mov_b32 exec_hi, 0xff00ff\n"
    "global_load_dwordx2 v[144:145], v192, s[16:17] sc1\n"
    "s_mov_b64 exec, -1\n"
    "s_sub_i32 s48, s48, 1\n"
    "s_cmp_gt_i32 s48, 0\n"
    "s_cselect_b64 s[52:53], -1, 0\n"
    "s_waitcnt vmcnt(0)\n"
    "v_cmp_ne_u32 vcc, s38, v145\n"
    "s_and_b64 vcc, vcc, s[52:53]\n"
    "s_cbranch_scc1 .Ltw55\n"
    "global_load_dwordx4 v[80:83], v191, s[16:17] offset:-4096 sc1\n"
    "global_load_dwordx4 v[84:87], v191, s[16:17] offset:-3072 sc1\n"
    "global_load_dwordx4 v[88:91], v191, s[16:17] offset:-2048 sc1\n"
    "global_load_dwordx4 v[92:95], v191, s[16:17] offset:-1024 sc1\n"
    "global_load_dwordx4 v[96:99], v191, s[16:17] offset:0 sc1\n"
    "global_load_dwordx4 v[100:103], v191, s[16:17] offset:1024 sc1\n"
    "global_load_dwordx4 v[104:107], v191, s[16:17] offset:2048 sc1\n"
    "global_load_dwordx4 v[108:111], v191, s[16:17] offset:3072 sc1\n"
    "s_mov_b32 exec_lo, 0xff00ff\n"
    "s_mov_b32 exec_hi, 0xff00ff\n"
    "global_load_dwordx2 v[144:145], v192, s[16:17] sc1\n"
    "s_mov_b64 exec, -1\n"
    "s_branch .Log53\n"
    ".Lhr58:\n"
    "v_lshl_add_u32 v198, v1, 7, s58\n"
    "s_mov_b64 exec, 0xff\n"
    ".Lhl60:\n"
    "global_load_dwordx2 v[200:201], v198, s[16:17] sc1\n"
    "s_sub_i32 s48, s48, 1\n"
    "s_cmp_gt_i32 s48, 0\n"
    "s_cselect_b64 s[52:53], -1, 0\n"
    "s_waitcnt vmcnt(0)\n"
    "v_cmp_ne_u32 vcc, s38, v201\n"
    "s_and_b64 vcc, vcc, s[52:53]\n"
    "s_cbranch_scc1 .Lhl60\n"
    "s_mov_b64 exec, -1\n"
    "v_subrev_u32 v200, s10, v200\n"
    "s_nop 1\n"
    "v_max_i32_dpp v200, v200, v200 quad_perm:[1,0,3,2] row_mask:0xf bank_mask:0xf bound_ctrl:1\n"
    "s_nop 1\n"
    "v_max_i32_dpp v200, v200, v200 quad_perm:[2,3,0,1] row_mask:0xf bank_mask:0xf bound_ctrl:1\n"
    "s_nop 1\n"
    "v_max_i32_dpp v200, v200, v200 row_half_mirror row_mask:0xf bank_mask:0xf bound_ctrl:1\n"
    "s_nop 0\n"
    "v_readlane_b32 s71, v200, 0\n"
    "s_max_i32 s71, s71, 0\n"
    "s_min_i32 s71, s71, 250\n"
    "s_branch .Lhb59\n"
    ".Lsr65:\n"
    "s_mov_b64 exec, 0xff\n"
    "global_load_dwordx4 v[48:51], v61, s[16:17] sc1\n"
    "s_mov_b64 exec, -1\n"
    "global_load_dwordx4 v[16:19], v60, s[16:17] offset:-4096 sc1\n"
    "global_load_dwordx4 v[20:23], v60, s[16:17] offset:-3072 sc1\n"
    "global_load_dwordx4 v[24:27], v60, s[16:17] offset:-2048 sc1\n"
    "global_load_dwordx4 v[28:31], v60, s[16:17] offset:-1024 sc1\n"
    "global_load_dwordx4 v[32:35], v60, s[16:17] offset:0 sc1\n"
    "global_load_dwordx4 v[36:39], v60, s[16:17] offset:1024 sc1\n"
    "global_load_dwordx4 v[40:43], v60, s[16:17] offset:2048 sc1\n"
    "global_load_dwordx4 v[44:47], v60, s[16:17] offset:3072 sc1\n"
    "s_mov_b32 exec_lo, s74\n"
    "s_mov_b32 exec_hi, 0\n"
    "global_load_dwordx2 v[74:75], v198, s[16:17] sc1\n"
    "s_mov_b64 exec, -1\n"
    "s_branch .Lst64\n"
    ".Lcr71:\n"
    "s_mov_b32 exec_hi, 0\n"
    "global_load_dwordx2 v[74:75], v198, s[16:17] sc1\n"
    "s_mov_b32 exec_hi, -1\n"
    "s_sub_i32 s48, s48, 1\n"
    "s_cmp_gt_i32 s48, 0\n"
    "s_cselect_b64 s[52:53], -1, 0\n"
    "s_waitcnt vmcnt(0)\n"
    "v_cmp_ne_u32 vcc, s38, v75\n"
    "s_and_b64 vcc, vcc, s[52:53]\n"
    "s_cbranch_scc1 .Lcr71\n"
    "v_readfirstlane_b32 s70, v74\n"
    "s_nop 1\n"
    "v_subrev_u32 v74, s70, v74\n"
    "s_nop 1\n"
    "v_max_i32_dpp v74, v74, v74 row_ror:8 row_mask:0xf bank_mask:0xf bound_ctrl:1\n"
    "s_nop 1\n"
    "v_max_i32_dpp v74, v74, v74 row_ror:4 row_mask:0xf bank_mask:0xf bound_ctrl:1\n"
    "s_nop 1\n"
    "v_max_i32_dpp v74, v74, v74 row_ror:2 row_mask:0xf bank_mask:0xf bound_ctrl:1\n"
    "s_nop 1\n"
    "v_max_i32_dpp v74, v74, v74 row_ror:1 row_mask:0xf bank_mask:0xf bound_ctrl:1\n"
    "s_nop 0\n"
    "v_readlane_b32 s71, v74, 0\n"
    "v_readlane_b32 s54, v74, 16\n"
    "s_max_i32 s71, s71, s54\n"
    "s_max_i32 s71, s71, 0\n"
    "s_min_i32 s71, s71, 250\n"
    "s_add_u32 s70, s70, s71\n"
    "s_add_u32 s71, s70, 60\n"
    "s_branch .Lch70\n"
    ".Lcp69:\n"
    "global_load_dwordx4 v[144:147], v84, s[16:17] sc1\n"
    "s_sub_i32 s48, s48, 1\n"
    "s_cmp_gt_i32 s48, 0\n"
    "s_cselect_b64 s[52:53], -1, 0\n"
    "s_waitcnt vmcnt(0)\n"
    "s_branch .Lcq73\n"
    ".Lhr78:\n"
    "global_load_dwordx4 v[88:91], v242, s[16:17] offset:0 sc1\n"
    "global_load_dwordx4 v[92:95], v242, s[16:17] offset:1024 sc1\n"
    "global_load_dwordx4 v[96:99], v243, s[16:17] offset:0 sc1\n"
    "global_load_dwordx4 v[100:103], v243, s[16:17] offset:1024 sc1\n"
    "s_sub_i32 s48, s48, 1\n"
    "s_cmp_gt_i32 s48, 0\n"
    "s_cselect_b64 s[52:53], -1, 0\n"
    "s_branch .Lhp77\n"
    ".Lcr88:\n"
    "s_mov_b32 exec_hi, 0\n"
    "global_load_dwordx2 v[74:75], v198, s[16:17] sc1\n"
    "s_mov_b32 exec_hi, -1\n"
    "s_sub_i32 s48, s48, 1\n"
    "s_cmp_gt_i32 s48, 0\n"
    "s_cselect_b64 s[52:53], -1, 0\n"
    "s_branch .Lch87\n"
    ".Lkend:\n"
    :: "{s[0:1]}"(__builtin_amdgcn_kernarg_segment_ptr()), "{s2}"(blockIdx.x), "{v0}"(threadIdx.x)
    : "v1", "v2", "v3", "v4", "v5", "v6", "v7", "v8", "v9", "v10", "v11", "v12", "v13", "v14", "v15", "v16", "v17", "v18", "v19", "v20", "v21", "v22", "v23", "v24", "v25", "v26", "v27", "v28", "v29", "v30", "v31", "v32", "v33", "v34", "v35", "v36", "v37", "v38", "v39", "v40", "v41", "v42", "v43", "v44", "v45", "v46", "v47", "v48", "v49", "v50", "v51", "v52", "v53", "v54", "v55", "v56", "v57", "v58", "v59", "v60", "v61", "v62", "v63", "v64", "v65", "v66", "v67", "v68", "v69", "v70", "v71", "v72", "v73", "v74", "v75", "v76", "v77", "v78", "v79", "v80", "v81", "v82", "v83", "v84", "v85", "v86", "v87", "v88", "v89", "v90", "v91", "v92", "v93", "v94", "v95", "v96", "v97", "v98", "v99", "v100", "v101", "v102", "v103", "v104", "v105", "v106", "v107", "v108", "v109", "v110", "v111", "v112", "v113", "v114", "v115", "v116", "v117", "v118", "v119", "v120", "v121", "v122", "v123", "v124", "v125", "v126", "v127", "v128", "v129", "v130", "v131", "v132", "v133", "v134", "v135", "v136", "v137", "v138", "v139", "v140", "v141", "v142", "v143", "v144", "v145", "v146", "v147", "v148", "v149", "v150", "v151", "v152", "v153", "v154", "v155", "v156", "v157", "v158", "v159", "v160", "v161", "v162", "v163", "v164", "v165", "v166", "v167", "v168", "v169", "v170", "v171", "v172", "v173", "v174", "v175", "v176", "v177", "v178", "v179", "v180", "v181", "v182", "v183", "v184", "v185", "v186", "v187", "v188", "v189", "v190", "v191", "v192", "v193", "v194", "v195", "v196", "v197", "v198", "v199", "v200", "v201", "v202", "v203", "v204", "v205", "v206", "v207", "v208", "v209", "v210", "v211", "v212", "v213", "v214", "v215", "v216", "v217", "v218", "v219", "v220", "v221", "v222", "v223", "v224", "v225", "v226", "v227", "v228", "v229", "v230", "v231", "v232", "v233", "v234", "v235", "v236", "v237", "v238", "v239", "v240", "v241", "v242", "v243", "v244", "v245", "v246", "v247", "v248", "v249", "v250", "v251", "v252", "v253", "v254", "v255", "s3", "s4", "s5", "s6", "s7", "s8", "s9", "s10", "s11", "s12", "s13", "s14", "s15", "s16", "s17", "s18", "s19", "s20", "s21", "s22", "s23", "s24", "s25", "s26", "s27", "s28", "s29", "s30", "s31", "s33", "s34", "s35", "s36", "s37", "s38", "s39", "s40", "s41", "s42", "s43", "s44", "s45", "s46", "s47", "s48", "s49", "s50", "s51", "s52", "s53", "s54", "s55", "s56", "s57", "s58", "s59", "s60", "s61", "s62", "s63", "s64", "s65", "s66", "s67", "s68", "s69", "s70", "s71", "s72", "s73", "s74", "s75", "s76", "s77", "s78", "s79", "s80", "s81", "s82", "s83", "s84", "s85", "s86", "s87", "s88", "s89", "s90", "s91", "s92", "s93", "s94", "s95", "s96", "s97", "s98", "s99", "vcc", "scc", "memory");
}

extern "C" void kernel_launch(void* const* d_in, const int* in_sizes, int n_in, void* d_out, int out_size, void* d_ws, size_t ws_size,
                              hipStream_t stream) {
  fused_kernel<<<256, 512, 0, stream>>>(d_in[0], d_in[1], d_in[9], d_in[8], d_ws, d_in[2], d_in[4], d_out, d_in[5], d_in[6], d_in[7]);
}
```
